```python
import torch

def _softmax(x, axis=-1):
    return torch.softmax(x, dim=axis)
def _gelu(x):
    return torch.nn.functional.gelu(x, approximate="tanh")
def _silu(x):
    return torch.nn.functional.silu(x)
def _sigmoid(x):
    return torch.sigmoid(x)
def _layernorm(x, axis=-1, eps=1e-5):
    m = x.mean(dim=axis, keepdim=True)
    v = x.var(dim=axis, keepdim=True, unbiased=False)
    return (x - m) / torch.sqrt(v + eps)
def _scatter_add(tgt, idx, src, axis=0):
    return tgt.clone().index_add_(axis, idx, src)
def _segment_sum(src, seg, n):
    out = torch.zeros((n,) + src.shape[1:], dtype=src.dtype)
    return out.index_add_(0, seg, src)
def _pad(x, pw):
    flat = tuple(v for pair in reversed(pw) for v in pair)
    return torch.nn.functional.pad(x, flat)

kernel_name = 'gated_mix__ssm_conv_h3__dense_activation_mish__prompt'

def setup_inputs():

    _w = (lambda a: (lambda t: t.to(torch.bfloat16) if t.is_floating_point() else t)(torch.as_tensor(a)))
    def _A_setup():
        B, S, D = 1, 2048, 512
        ks = 4
        return {
            "q": _w((lambda *s: torch.randn(*s, dtype=torch.bfloat16) * 0.05)(B, S, D)),
            "k": _w((lambda *s: torch.randn(*s, dtype=torch.bfloat16) * 0.2)(B, S, D)),
            "v": _w((lambda *s: torch.randn(*s, dtype=torch.bfloat16) * 0.2)(B, S, D)),
            "shift_k": _w((lambda *s: torch.randn(*s, dtype=torch.bfloat16) * 0.2)(D, ks) * 0.5),
            "ssm_k": _w((lambda *s: torch.randn(*s, dtype=torch.bfloat16) * 0.8)(D, S) * (1.0 / S ** 0.5)),
            "ssm_D": _w((lambda *s: torch.randn(*s, dtype=torch.bfloat16) * 0.2)(D)),
        }
    _A_in = _A_setup()

    def _B_setup():
        B, S, D, F = 1, 2048, 512, 1376
        return {
            "x": _w((lambda *s: torch.randn(*s, dtype=torch.bfloat16) * 0.05)(B, S, D)),
            "w1": _w((lambda *s: torch.randn(*s, dtype=torch.bfloat16) * 3.2)(D, F) * (D ** -0.5)),
            "b1": _w((lambda *s: torch.randn(*s, dtype=torch.bfloat16) * 0.05)(F)),
            "w2": _w((lambda *s: torch.randn(*s, dtype=torch.bfloat16) * 0.05)(F, D) * (F ** -0.5)),
        }
    _B_in = _B_setup()

    return {
        "A_q": _A_in["q"],
        "A_k": _A_in["k"],
        "A_v": _A_in["v"],
        "A_shift_k": _A_in["shift_k"],
        "A_ssm_k": _A_in["ssm_k"],
        "A_ssm_D": _A_in["ssm_D"],
        "B_x": _B_in["x"],
        "B_w1": _B_in["w1"],
        "B_b1": _B_in["b1"],
        "B_w2": _B_in["w2"],
        "gate_w": _w((lambda *s: torch.randn(*s, dtype=torch.bfloat16) * 0.05)(512) * 16.0),
    }

def reference(A_q, A_k, A_v, A_shift_k, A_ssm_k, A_ssm_D, B_x, B_w1, B_b1, B_w2, gate_w):
    def _A_ref(q, k, v, shift_k, ssm_k, ssm_D):
        B, S, D = q.shape
        ks = shift_k.shape[-1]
        k_pad = _pad(k, ((0, 0), (ks - 1, 0), (0, 0)))
        k_shift = torch.zeros_like(k)
        for t in range(ks):
            k_shift = k_shift + k_pad[:, t:t + S, :] * shift_k[:, ks - 1 - t][None, None, :]
        kv = k_shift * v
        L = 2 * S
        kv_pad = _pad(kv, ((0, 0), (0, S), (0, 0)))
        kf_pad = _pad(ssm_k, ((0, 0), (0, S)))
        kv_f = (lambda x, axes=(-2,-1): torch.fft.rfft2(x.float(), dim=axes))(kv_pad[:, None, :, :], axes=(1, 2))
        kf_f = (lambda x, axes=(-2,-1): torch.fft.rfft2(x.float(), dim=axes))(kf_pad[:, None, :], axes=(1, 2))
        kf_f = (lambda x, *d: x.permute(*d))(kf_f, 1, 2, 0)
        y_f = kv_f * kf_f[None, :, :, :]
        y = (lambda x, s, axes=(-2,-1): torch.fft.irfft2(x, s=s, dim=axes).to(torch.bfloat16))(y_f, (1, L), axes=(1, 2))[:, 0, :S, :]
        y = y + kv * ssm_D[None, None, :]
        return q * y

    def _B_ref(x, w1, b1, w2):
        h = torch.einsum("bsd,df->bsf", x, w1) + b1
        sp = torch.log1p(torch.exp(-torch.abs(h))) + torch.where(h > 0, h, torch.zeros_like(h))
        act = h * torch.tanh(sp)
        return torch.einsum("bsf,fd->bsd", act, w2)

    x = A_q
    g = _sigmoid((x * gate_w).sum(-1, keepdims=True))
    a = _A_ref(x, A_k, A_v, A_shift_k, A_ssm_k, A_ssm_D)
    b = _B_ref(x, B_w1, B_b1, B_w2)
    return g * a + (1 - g) * b
```

```cpp
#include <hip/hip_runtime.h>
#include <hip/hip_bf16.h>
#include <stdint.h>

#define SEQ   2048
#define DM    512
#define FF    1376
#define NOCT  172
#define NOCTP 176

typedef unsigned short u16;
typedef __attribute__((ext_vector_type(8))) short bf16x8;
typedef __attribute__((ext_vector_type(4))) short s16x4;
typedef __attribute__((ext_vector_type(16))) float f32x16;
typedef __attribute__((ext_vector_type(4))) float f32x4;
typedef __attribute__((ext_vector_type(4))) unsigned int u32x4;
typedef __attribute__((ext_vector_type(2))) unsigned int u32x2;

struct Params {
  const u16* q; const u16* k; const u16* v; const u16* shk; const u16* ssmk; const u16* ssmD;
  const u16* w1; const u16* b1; const u16* w2; const u16* gw;
  u16* out;
  u16* uT; u16* kvN; u16* yT; u16* actF; u16* w2F;
  float* g; unsigned* flags;
};

__device__ __forceinline__ unsigned pk2(float a, float b) {
  typedef __attribute__((ext_vector_type(2))) __bf16 bf2;
  typedef __attribute__((ext_vector_type(2))) float f2;
  f2 v = {a, b};
  bf2 r = __builtin_convertvector(v, bf2);
  return __builtin_bit_cast(unsigned, r);
}
__device__ __forceinline__ float bflo(unsigned u) { return __builtin_bit_cast(float, u << 16); }
__device__ __forceinline__ float bfhi(unsigned u) { return __builtin_bit_cast(float, u & 0xffff0000u); }
__device__ __forceinline__ float rbf(float x) { return bflo(pk2(x, x) & 0xffffu); }
__device__ __forceinline__ float bfv(const u32x4& w, int i) {
  unsigned d = w[i >> 1];
  return (i & 1) ? bfhi(d) : bflo(d);
}

#define LDS_PTR(p) ((__attribute__((address_space(3))) void*)(p))

__device__ __forceinline__ void glds16(const void* gsrc, void* ldst) {
  __builtin_amdgcn_global_load_lds((const __attribute__((address_space(1))) void*)gsrc,
                                   (__attribute__((address_space(3))) void*)ldst, 16, 0, 0);
}

#define NSTG 4
#define XSTG 16384
#define WSTG 12288
#define K1_SIDE_OFF (NSTG * (XSTG + WSTG))
#define K1_LDS (K1_SIDE_OFF + 64 * 144)

__global__ __launch_bounds__(256, 1) void k1_kernel(Params p) {
  __shared__ __attribute__((aligned(16))) char smem[K1_LDS];
  const int tid = threadIdx.x, lane = tid & 63, wv = tid >> 6;
  const int w = blockIdx.x;
  const int grp = w & 7, idx = w >> 3;
  const int rb = 2 * grp + (idx >> 4);
  const int jt = idx & 15;
  const int oct0 = jt < 12 ? 11 * jt : 132 + 10 * (jt - 12);
  const int noct = jt < 12 ? 11 : 10;

  char* xs = smem;
  char* ws = smem + NSTG * XSTG;

  const int tb = w >> 3, cb = w & 7;
  const int so = tid & 7, rg = tid >> 3;
  const int t0 = tb * 64;
  u32x4 kr[5], vr[2], skr[4];
#pragma unroll
  for (int i = 0; i < 5; ++i) {
    int row = t0 + 2 * rg - 3 + i;
    if (row >= 0) kr[i] = *(const u32x4*)(p.k + (size_t)row * DM + cb * 64 + so * 8);
    else kr[i] = (u32x4){0u, 0u, 0u, 0u};
  }
#pragma unroll
  for (int i = 0; i < 2; ++i)
    vr[i] = *(const u32x4*)(p.v + (size_t)(t0 + 2 * rg + i) * DM + cb * 64 + so * 8);
#pragma unroll
  for (int i = 0; i < 4; ++i)
    skr[i] = *(const u32x4*)(p.shk + (size_t)(cb * 64 + so * 8) * 4 + i * 8);
  u32x4 xg[2], gwr;
#pragma unroll
  for (int i = 0; i < 2; ++i)
    xg[i] = *(const u32x4*)(p.q + (size_t)(8 * w + 2 * wv + i) * DM + lane * 8);
  gwr = *(const u32x4*)(p.gw + lane * 8);

  const u16* xsrc[4];
#pragma unroll
  for (int i = 0; i < 4; ++i) {
    int off = (wv * 4 + i) * 1024 + lane * 16;
    int row = off >> 7, pc = (off & 127) >> 4;
    int lc = pc ^ ((row >> 1) & 7);
    xsrc[i] = p.q + (size_t)(rb * 128 + row) * DM + lc * 8;
  }
  const u16* wsrc[3];
#pragma unroll
  for (int i = 0; i < 3; ++i) {
    int off = (wv * 3 + i) * 1024 + lane * 16;
    int row = off / 192, col = (off - row * 192) >> 1;
    int gc = oct0 * 8 + col;
    if (gc > FF - 8) gc = FF - 8;
    wsrc[i] = p.w1 + (size_t)row * FF + gc;
  }
  auto issue = [&](int s) {
    char* xd = xs + (s & (NSTG - 1)) * XSTG + wv * 4096 + lane * 16;
    char* wd = ws + (s & (NSTG - 1)) * WSTG + wv * 3072 + lane * 16;
#pragma unroll
    for (int i = 0; i < 4; ++i) glds16(xsrc[i] + s * 64, xd + i * 1024);
#pragma unroll
    for (int i = 0; i < 3; ++i) glds16(wsrc[i] + (size_t)s * 64 * FF, wd + i * 1024);
  };
  issue(0); issue(1); issue(2);

  {
    char* sb = smem + K1_SIDE_OFF;
    float kvv[2][8];
#pragma unroll
    for (int rr = 0; rr < 2; ++rr) {
#pragma unroll
      for (int c = 0; c < 8; ++c) {
        const u32x4& sk = skr[c >> 1];
        int b0 = (c & 1) * 4;
        float s0 = bfv(sk, b0 + 0), s1 = bfv(sk, b0 + 1), s2 = bfv(sk, b0 + 2), s3 = bfv(sk, b0 + 3);
        float acc = rbf(bfv(kr[rr + 0], c) * s3);
        acc = rbf(acc + rbf(bfv(kr[rr + 1], c) * s2));
        acc = rbf(acc + rbf(bfv(kr[rr + 2], c) * s1));
        acc = rbf(acc + rbf(bfv(kr[rr + 3], c) * s0));
        kvv[rr][c] = rbf(acc * bfv(vr[rr], c));
      }
    }
#pragma unroll
    for (int rr = 0; rr < 2; ++rr) {
      u32x4 o;
#pragma unroll
      for (int c = 0; c < 4; ++c) o[c] = pk2(kvv[rr][2 * c], kvv[rr][2 * c + 1]);
      *(u32x4*)(p.kvN + (size_t)(t0 + 2 * rg + rr) * DM + cb * 64 + so * 8) = o;
    }
#pragma unroll
    for (int c = 0; c < 8; ++c)
      *(unsigned*)(sb + (so * 8 + c) * 144 + rg * 4) = pk2(kvv[0][c], kvv[1][c]);
    __syncthreads();
    {
      int ch = tid >> 2, qt = tid & 3;
      u32x4 a = *(const u32x4*)(sb + ch * 144 + qt * 32);
      u32x4 b = *(const u32x4*)(sb + ch * 144 + qt * 32 + 16);
      u16* dst = p.uT + (size_t)(cb * 64 + ch) * SEQ + t0 + qt * 16;
      *(u32x4*)dst = a;
      *(u32x4*)(dst + 8) = b;
    }
#pragma unroll
    for (int i = 0; i < 2; ++i) {
      float s = 0.f;
#pragma unroll
      for (int c = 0; c < 8; ++c) s += rbf(bfv(xg[i], c) * bfv(gwr, c));
#pragma unroll
      for (int m = 32; m >= 1; m >>= 1) s += __shfl_xor(s, m);
      s = rbf(s);
      float gg = rbf(1.0f / (1.0f + __expf(-s)));
      if (lane == 0) p.g[8 * w + 2 * wv + i] = gg;
    }
    for (int blk = w * 4 + wv; blk < 16 * NOCTP; blk += 1024) {
      int nb = blk / NOCTP, o = blk - nb * NOCTP;
      int n = lane & 31, hh = lane >> 5;
      u32x2 val = {0u, 0u};
      if (o < NOCT) {
        const u16* src = p.w2 + (size_t)(o * 8 + hh * 4) * DM + nb * 32 + n;
        unsigned e0 = src[0], e1 = src[DM], e2 = src[2 * DM], e3 = src[3 * DM];
        val[0] = e0 | (e1 << 16);
        val[1] = e2 | (e3 << 16);
      }
      *(u32x2*)(p.w2F + (size_t)blk * 256 + n * 8 + hh * 4) = val;
    }
    if (wv == 0 && lane < 32) {
      int R = w >> 2, o = NOCT + (w & 3);
      *(u32x4*)(p.actF + ((size_t)(R * NOCTP + o) * 32 + lane) * 8) = (u32x4){0u, 0u, 0u, 0u};
    }
    if (tid == 0) p.flags[w * 16] = 0u;
  }

  const int r = lane & 31, h = lane >> 5;
  f32x16 acc[3];
#pragma unroll
  for (int c = 0; c < 3; ++c)
#pragma unroll
    for (int i = 0; i < 16; ++i) acc[c][i] = 0.f;

  const int xrow = wv * 32 + r;
  const int xsw = (xrow >> 1) & 7;
  const int g16 = lane >> 4, q4 = (lane & 15) >> 2, pp = lane & 3;
  const int trrow = 8 * (g16 >> 1) + q4, trcol = 16 * (g16 & 1) + 4 * pp;

#pragma unroll
  for (int s = 0; s < 8; ++s) {
    if (s <= 4) asm volatile("s_waitcnt vmcnt(21)" ::: "memory");
    else if (s == 5) asm volatile("s_waitcnt vmcnt(14)" ::: "memory");
    else if (s == 6) asm volatile("s_waitcnt vmcnt(7)" ::: "memory");
    else asm volatile("s_waitcnt vmcnt(0)" ::: "memory");
    __builtin_amdgcn_s_barrier();
    if (s + 3 < 8) issue(s + 3);
    const char* xb = xs + (s & (NSTG - 1)) * XSTG + xrow * 128;
    const char* wb = ws + (s & (NSTG - 1)) * WSTG;
#pragma unroll
    for (int ks = 0; ks < 4; ++ks) {
      bf16x8 xf = *(const bf16x8*)(xb + (((2 * ks + h) ^ xsw) << 4));
#pragma unroll
      for (int ct = 0; ct < 3; ++ct) {
        const char* a0 = wb + (16 * ks + trrow) * 192 + (32 * ct + trcol) * 2;
        s16x4 lo = __builtin_amdgcn_ds_read_tr16_b64_v4i16((__attribute__((address_space(3))) s16x4*)a0);
        s16x4 hi = __builtin_amdgcn_ds_read_tr16_b64_v4i16((__attribute__((address_space(3))) s16x4*)(a0 + 4 * 192));
        bf16x8 wf = __builtin_shufflevector(lo, hi, 0, 1, 2, 3, 4, 5, 6, 7);
        acc[ct] = __builtin_amdgcn_mfma_f32_32x32x16_bf16(wf, xf, acc[ct], 0, 0, 0);
      }
    }
  }

  const int R = rb * 4 + wv;
#pragma unroll
  for (int ct = 0; ct < 3; ++ct) {
#pragma unroll
    for (int gq = 0; gq < 4; ++gq) {
      int ol = 4 * ct + gq;
      if (ol < noct) {
        int o = oct0 + ol;
        int f = o * 8 + 4 * h;
        u32x2 bb = *(const u32x2*)(p.b1 + f);
        float bv[4] = {bflo(bb[0]), bfhi(bb[0]), bflo(bb[1]), bfhi(bb[1])};
        float res[4];
#pragma unroll
        for (int i = 0; i < 4; ++i) {
          float hv = rbf(rbf(acc[ct][4 * gq + i]) + bv[i]);
          float e = __expf(fminf(hv, 30.f));
          float n = e * (e + 2.f);
          res[i] = hv * n * __builtin_amdgcn_rcpf(n + 2.f);
        }
        u32x2 o2 = {pk2(res[0], res[1]), pk2(res[2], res[3])};
        *(u32x2*)((char*)p.actF + (size_t)(R * NOCTP + o) * 512 + r * 16 + h * 8) = o2;
      }
    }
  }
}

#define CP_STRIDE 4160
#define CONV_U_OFF (2 * 8 * CP_STRIDE)
#define U_CH_STRIDE 7600
#define K2_LDS (CONV_U_OFF + 2 * U_CH_STRIDE)

__device__ __forceinline__ void conv_wg(const Params& p, int c, char* smem) {
  const int tid = threadIdx.x, lane = tid & 63, wv = tid >> 6;
  char* cps = smem;
  char* us = smem + CONV_U_OFF;

#pragma unroll
  for (int i = 0; i < 2; ++i) {
    int x = tid + i * 256;
    int ch = x >> 8, xx = x & 255;
    u32x4 val = *(const u32x4*)(p.uT + (size_t)(2 * c + ch) * SEQ + xx * 8);
    *(u32x4*)(us + ch * U_CH_STRIDE + ((xx >> 2) + 31) * 80 + (xx & 3) * 16) = val;
  }
  for (int z = tid; z < 2 * 155; z += 256) {
    int ch = z / 155, zz = z - ch * 155;
    *(u32x4*)(us + ch * U_CH_STRIDE + zz * 16) = (u32x4){0u, 0u, 0u, 0u};
  }
  for (int it = tid; it < 2 * 259; it += 256) {
    int ch = it / 259, t = it - ch * 259;
    const u16* f = p.ssmk + (size_t)(2 * c + ch) * SEQ;
    int i0 = 8 * t - 32;
    u32x4 lo = (u32x4){0u, 0u, 0u, 0u}, hi = (u32x4){0u, 0u, 0u, 0u};
    if (i0 >= 0) lo = *(const u32x4*)(f + i0);
    if (i0 + 8 >= 0 && i0 + 8 < SEQ) hi = *(const u32x4*)(f + i0 + 8);
    unsigned wd[8] = {lo[0], lo[1], lo[2], lo[3], hi[0], hi[1], hi[2], hi[3]};
    unsigned rp[14];
#pragma unroll
    for (int i = -6; i <= 7; ++i) {
      if (i & 1) { unsigned x = wd[(i + 7) / 2]; rp[i + 6] = (x >> 16) | (x << 16); }
      else { rp[i + 6] = (wd[(i + 8) / 2] & 0xffffu) | (wd[(i + 8) / 2 - 1] & 0xffff0000u); }
    }
#pragma unroll
    for (int cc = 0; cc < 8; ++cc) {
      u32x4 o = {rp[cc + 6], rp[cc - 2 + 6], rp[cc - 4 + 6], rp[cc - 6 + 6]};
      *(u32x4*)(cps + (ch * 8 + cc) * CP_STRIDE + t * 16) = o;
    }
  }
  __syncthreads();

  const int ch = wv >> 1, isB = wv & 1;
  const int r = lane & 31, h = lane >> 5;
  const char* ab = cps + (ch * 8 + (r & 7)) * CP_STRIDE + 16 * ((r >> 3) - h + 3);
  const char* ub = us + ch * U_CH_STRIDE + (r + 31) * 80 + 16 * h;

  f32x16 acc0, acc1;
#pragma unroll
  for (int i = 0; i < 16; ++i) { acc0[i] = 0.f; acc1[i] = 0.f; }

  {
    const int tau = isB ? 0 : 1;
    const int qe = isB ? 32 : 48;
    const char* a = ab;
    const char* b = ub + tau * 32 * 80;
#pragma unroll 4
    for (int qq = 0; qq < qe; ++qq) {
      bf16x8 a0 = *(const bf16x8*)(a - 32);
      bf16x8 b0 = *(const bf16x8*)(b + 32);
      bf16x8 a1 = *(const bf16x8*)(a);
      bf16x8 b1 = *(const bf16x8*)(b);
      acc0 = __builtin_amdgcn_mfma_f32_32x32x16_bf16(a0, b0, acc0, 0, 0, 0);
      acc0 = __builtin_amdgcn_mfma_f32_32x32x16_bf16(a1, b1, acc0, 0, 0, 0);
      a += 64; b -= 80;
    }
  }
  if (isB) {
    const char* a = ab + 48 * 64;
    const char* b = ub + 32 * 80 - 48 * 80;
#pragma unroll 4
    for (int qq = 48; qq < 64; ++qq) {
      bf16x8 a0 = *(const bf16x8*)(a - 32);
      bf16x8 b0 = *(const bf16x8*)(b + 32);
      bf16x8 a1 = *(const bf16x8*)(a);
      bf16x8 b1 = *(const bf16x8*)(b);
      acc1 = __builtin_amdgcn_mfma_f32_32x32x16_bf16(a0, b0, acc1, 0, 0, 0);
      acc1 = __builtin_amdgcn_mfma_f32_32x32x16_bf16(a1, b1, acc1, 0, 0, 0);
      a += 64; b -= 80;
    }
  }
  __syncthreads();
  float* red = (float*)(smem + ch * 4096);
  if (isB) {
#pragma unroll
    for (int i = 0; i < 16; ++i) red[i * 64 + lane] = acc1[i];
  }
  __syncthreads();
  if (!isB) {
#pragma unroll
    for (int i = 0; i < 16; ++i) acc0[i] += red[i * 64 + lane];
  }
  u16* ost = (u16*)(smem + 8192) + ch * SEQ;
  {
    const int tau = isB ? 0 : 1;
#pragma unroll
    for (int gq = 0; gq < 4; ++gq) {
      u32x2 o = {pk2(acc0[4 * gq], acc0[4 * gq + 1]), pk2(acc0[4 * gq + 2], acc0[4 * gq + 3])};
      *(u32x2*)(ost + 1024 * tau + 32 * r + 8 * gq + 4 * h) = o;
    }
  }
  __syncthreads();
  {
    auto rsrc = __builtin_amdgcn_make_buffer_rsrc((void*)p.yT, 0, DM * SEQ * 2, 0x00020000);
#pragma unroll
    for (int i = 0; i < 2; ++i) {
      int x = tid + i * 256;
      int chh = x >> 8, xx = x & 255;
      u32x4 val = *(const u32x4*)((const u16*)(smem + 8192) + chh * SEQ + xx * 8);
      __builtin_amdgcn_raw_buffer_store_b128(val, rsrc, ((2 * c + chh) * SEQ + xx * 8) * 2, 0, 16);
    }
  }
  asm volatile("s_waitcnt vmcnt(0)" ::: "memory");
  __syncthreads();
  if (tid == 0) __hip_atomic_store(p.flags + c * 16, 1u, __ATOMIC_RELAXED, __HIP_MEMORY_SCOPE_AGENT);
}

#define RED_PITCH 68
__device__ __forceinline__ void gemm2_wg(const Params& p, int gi, char* smem) {
  const int tid = threadIdx.x, lane = tid & 63, wv = tid >> 6;
  const int grp = gi & 7, idx = gi >> 3;
  const int rb = 4 * grp + (idx >> 3);
  const int cb = idx & 7;

  const int erow = tid >> 2, ecol = (tid & 3) * 16;
  const int s = rb * 64 + erow, d0 = cb * 64 + ecol;
  u32x4 qv[2], kvv[2], dv[2];
#pragma unroll
  for (int i = 0; i < 2; ++i) {
    qv[i] = *(const u32x4*)(p.q + (size_t)s * DM + d0 + i * 8);
    kvv[i] = *(const u32x4*)(p.kvN + (size_t)s * DM + d0 + i * 8);
    dv[i] = *(const u32x4*)(p.ssmD + d0 + i * 8);
  }
  float gg = p.g[s];

  f32x16 acc[2][2];
#pragma unroll
  for (int a = 0; a < 2; ++a)
#pragma unroll
    for (int b = 0; b < 2; ++b)
#pragma unroll
      for (int i = 0; i < 16; ++i) acc[a][b][i] = 0.f;

  const char* ap = (const char*)p.actF + ((size_t)(2 * rb) * NOCTP + wv * 44) * 512 + lane * 16;
  const char* bp = (const char*)p.w2F + ((size_t)(2 * cb) * NOCTP + wv * 44) * 512 + lane * 16;
  constexpr int PF = 4;
  bf16x8 fa[PF][2], fb[PF][2];
#pragma unroll
  for (int i = 0; i < PF; ++i) {
    fa[i][0] = *(const bf16x8*)(ap + i * 1024);
    fa[i][1] = *(const bf16x8*)(ap + NOCTP * 512 + i * 1024);
    fb[i][0] = *(const bf16x8*)(bp + i * 1024);
    fb[i][1] = *(const bf16x8*)(bp + NOCTP * 512 + i * 1024);
  }
#pragma unroll
  for (int ks = 0; ks < 22; ++ks) {
    const int i = ks % PF;
    bf16x8 a0 = fa[i][0], a1 = fa[i][1], b0 = fb[i][0], b1 = fb[i][1];
    if (ks + PF < 22) {
      fa[i][0] = *(const bf16x8*)(ap + (ks + PF) * 1024);
      fa[i][1] = *(const bf16x8*)(ap + NOCTP * 512 + (ks + PF) * 1024);
      fb[i][0] = *(const bf16x8*)(bp + (ks + PF) * 1024);
      fb[i][1] = *(const bf16x8*)(bp + NOCTP * 512 + (ks + PF) * 1024);
    }
    acc[0][0] = __builtin_amdgcn_mfma_f32_32x32x16_bf16(a0, b0, acc[0][0], 0, 0, 0);
    acc[0][1] = __builtin_amdgcn_mfma_f32_32x32x16_bf16(a0, b1, acc[0][1], 0, 0, 0);
    acc[1][0] = __builtin_amdgcn_mfma_f32_32x32x16_bf16(a1, b0, acc[1][0], 0, 0, 0);
    acc[1][1] = __builtin_amdgcn_mfma_f32_32x32x16_bf16(a1, b1, acc[1][1], 0, 0, 0);
  }

  float* red = (float*)smem;
  const int r = lane & 31, h = lane >> 5;
#pragma unroll
  for (int a = 0; a < 2; ++a)
#pragma unroll
    for (int b = 0; b < 2; ++b)
#pragma unroll
      for (int i = 0; i < 16; ++i) {
        int m = a * 32 + (i & 3) + 8 * (i >> 2) + 4 * h;
        red[(wv * 64 + m) * RED_PITCH + b * 32 + r] = acc[a][b][i];
      }

  if (wv == 0) {
    const unsigned* fl = p.flags + (cb * 32 + (lane & 31)) * 16;
    for (int spin = 0; spin < (1 << 22); ++spin) {
      unsigned v = __hip_atomic_load(fl, __ATOMIC_RELAXED, __HIP_MEMORY_SCOPE_AGENT);
      if (__all(v != 0u)) break;
      __builtin_amdgcn_s_sleep(2);
    }
  }
  __syncthreads();
  __builtin_amdgcn_fence(__ATOMIC_ACQUIRE, "wavefront");

  u16* ys = (u16*)(smem + 4 * 64 * RED_PITCH * 4);
  {
    auto rsrc = __builtin_amdgcn_make_buffer_rsrc((void*)p.yT, 0, DM * SEQ * 2, 0x00020000);
    int dd = tid >> 2, qt = tid & 3;
    int goff = ((cb * 64 + dd) * SEQ + rb * 64 + qt * 16) * 2;
    u32x4 v0 = __builtin_amdgcn_raw_buffer_load_b128(rsrc, goff, 0, 16);
    u32x4 v1 = __builtin_amdgcn_raw_buffer_load_b128(rsrc, goff + 16, 0, 16);
    *(u32x4*)(ys + dd * 72 + qt * 16) = v0;
    *(u32x4*)(ys + dd * 72 + qt * 16 + 8) = v1;
  }
  __syncthreads();

  float bsum[16];
#pragma unroll
  for (int j = 0; j < 4; ++j) {
    f32x4 t = *(const f32x4*)(red + (0 * 64 + erow) * RED_PITCH + ecol + j * 4);
#pragma unroll
    for (int ww = 1; ww < 4; ++ww) t += *(const f32x4*)(red + (ww * 64 + erow) * RED_PITCH + ecol + j * 4);
#pragma unroll
    for (int i = 0; i < 4; ++i) bsum[j * 4 + i] = t[i];
  }
  const float omg = rbf(1.0f - gg);
  float res[16];
#pragma unroll
  for (int j = 0; j < 16; ++j) {
    float y = bflo((unsigned)ys[(ecol + j) * 72 + erow]);
    float kv = bfv(kvv[j >> 3], j & 7), dd = bfv(dv[j >> 3], j & 7), qq = bfv(qv[j >> 3], j & 7);
    float ysum = rbf(y + rbf(kv * dd));
    float a = rbf(qq * ysum);
    float b = rbf(bsum[j]);
    res[j] = rbf(gg * a) + rbf(omg * b);
  }
  u32x4 o0, o1;
#pragma unroll
  for (int j = 0; j < 4; ++j) { o0[j] = pk2(res[2 * j], res[2 * j + 1]); o1[j] = pk2(res[8 + 2 * j], res[8 + 2 * j + 1]); }
  *(u32x4*)(p.out + (size_t)s * DM + d0) = o0;
  *(u32x4*)(p.out + (size_t)s * DM + d0 + 8) = o1;
}

__global__ __launch_bounds__(256, 2) void k2_kernel(Params p) {
  __shared__ __attribute__((aligned(16))) char smem[K2_LDS];
  if (blockIdx.x < 256) conv_wg(p, blockIdx.x, smem);
  else gemm2_wg(p, blockIdx.x - 256, smem);
}

extern "C" void kernel_launch(void* const* d_in, const int* in_sizes, int n_in,
                              void* d_out, int out_size, void* d_ws, size_t ws_size,
                              hipStream_t stream) {
  Params p;
  p.q = (const u16*)d_in[0];
  p.k = (const u16*)d_in[1];
  p.v = (const u16*)d_in[2];
  p.shk = (const u16*)d_in[3];
  p.ssmk = (const u16*)d_in[4];
  p.ssmD = (const u16*)d_in[5];
  p.w1 = (const u16*)d_in[7];
  p.b1 = (const u16*)d_in[8];
  p.w2 = (const u16*)d_in[9];
  p.gw = (const u16*)d_in[10];
  p.out = (u16*)d_out;
  char* ws = (char*)d_ws;
  size_t off = 0;
  p.uT = (u16*)(ws + off);   off += (size_t)DM * SEQ * 2;
  p.kvN = (u16*)(ws + off);  off += (size_t)DM * SEQ * 2;
  p.yT = (u16*)(ws + off);   off += (size_t)DM * SEQ * 2;
  p.actF = (u16*)(ws + off); off += (size_t)64 * NOCTP * 512;
  p.w2F = (u16*)(ws + off);  off += (size_t)16 * NOCTP * 512;
  p.g = (float*)(ws + off);  off += SEQ * 4;
  p.flags = (unsigned*)(ws + off); off += 256 * 64;
  k1_kernel<<<256, 256, 0, stream>>>(p);
  k2_kernel<<<512, 256, 0, stream>>>(p);
}
```

```cpp
#include <hip/hip_runtime.h>
#include <hip/hip_bf16.h>
#include <stdint.h>

#define SEQ   2048
#define DM    512
#define FF    1376
#define NOCT  172
#define NOCTP 176

typedef unsigned short u16;
typedef __attribute__((ext_vector_type(8))) short bf16x8;
typedef __attribute__((ext_vector_type(4))) short s16x4;
typedef __attribute__((ext_vector_type(16))) float f32x16;
typedef __attribute__((ext_vector_type(4))) float f32x4;
typedef __attribute__((ext_vector_type(4))) unsigned int u32x4;
typedef __attribute__((ext_vector_type(2))) unsigned int u32x2;

struct Params {
  const u16* q; const u16* k; const u16* v; const u16* shk; const u16* ssmk; const u16* ssmD;
  const u16* w1; const u16* b1; const u16* w2; const u16* gw;
  u16* out;
  u16* uT; u16* kvN; u16* yT; u16* actF; u16* w2F;
  float* g; unsigned* flags;
};

__device__ __forceinline__ unsigned pk2(float a, float b) {
  typedef __attribute__((ext_vector_type(2))) __bf16 bf2;
  typedef __attribute__((ext_vector_type(2))) float f2;
  f2 v = {a, b};
  bf2 r = __builtin_convertvector(v, bf2);
  return __builtin_bit_cast(unsigned, r);
}
__device__ __forceinline__ float bflo(unsigned u) { return __builtin_bit_cast(float, u << 16); }
__device__ __forceinline__ float bfhi(unsigned u) { return __builtin_bit_cast(float, u & 0xffff0000u); }
__device__ __forceinline__ float rbf(float x) { return bflo(pk2(x, x) & 0xffffu); }
__device__ __forceinline__ float bfv(const u32x4& w, int i) {
  unsigned d = w[i >> 1];
  return (i & 1) ? bfhi(d) : bflo(d);
}

#define LDS_PTR(p) ((__attribute__((address_space(3))) void*)(p))

__device__ __forceinline__ void glds16(const void* gsrc, unsigned lds_dst) {
  unsigned keep;
  asm volatile("s_mov_b32 %0, m0\n\ts_mov_b32 m0, %2\n\ts_nop 0\n\tglobal_load_lds_dwordx4 %1, off\n\ts_mov_b32 m0, %0"
               : "=&s"(keep) : "v"(gsrc), "s"(lds_dst) : "memory");
}
#define SCHED_FENCE() __builtin_amdgcn_sched_barrier(0)

#define NSTG 4
#define XSTG 16384
#define WSTG 12288
#define K1_SIDE_OFF (NSTG * (XSTG + WSTG))
#define K1_LDS (K1_SIDE_OFF + 64 * 144)

__global__ __launch_bounds__(256, 1) void k1_kernel(Params p) {
  __shared__ __attribute__((aligned(16))) char smem[K1_LDS];
  const int tid = threadIdx.x, lane = tid & 63, wv = tid >> 6;
  const int w = blockIdx.x;
  const int grp = w & 7, idx = w >> 3;
  const int rb = 2 * grp + (idx >> 4);
  const int jt = idx & 15;
  const int oct0 = jt < 12 ? 11 * jt : 132 + 10 * (jt - 12);
  const int noct = jt < 12 ? 11 : 10;

  char* xs = smem;
  char* ws = smem + NSTG * XSTG;

  const int tb = w >> 3, cb = w & 7;
  const int so = tid & 7, rg = tid >> 3;
  const int t0 = tb * 64;
  u32x4 kr[5], vr[2], skr[4];
#pragma unroll
  for (int i = 0; i < 5; ++i) {
    int row = t0 + 2 * rg - 3 + i;
    int rc = row < 0 ? 0 : row;
    u32x4 t = *(const u32x4*)(p.k + (size_t)rc * DM + cb * 64 + so * 8);
    kr[i] = row < 0 ? (u32x4){0u, 0u, 0u, 0u} : t;
  }
#pragma unroll
  for (int i = 0; i < 2; ++i)
    vr[i] = *(const u32x4*)(p.v + (size_t)(t0 + 2 * rg + i) * DM + cb * 64 + so * 8);
#pragma unroll
  for (int i = 0; i < 4; ++i)
    skr[i] = *(const u32x4*)(p.shk + (size_t)(cb * 64 + so * 8) * 4 + i * 8);
  u32x4 xg[2], gwr;
#pragma unroll
  for (int i = 0; i < 2; ++i)
    xg[i] = *(const u32x4*)(p.q + (size_t)(8 * w + 2 * wv + i) * DM + lane * 8);
  gwr = *(const u32x4*)(p.gw + lane * 8);

  const u16* xsrc[4];
#pragma unroll
  for (int i = 0; i < 4; ++i) {
    int off = (wv * 4 + i) * 1024 + lane * 16;
    int row = off >> 7, pc = (off & 127) >> 4;
    int lc = pc ^ ((row >> 1) & 7);
    xsrc[i] = p.q + (size_t)(rb * 128 + row) * DM + lc * 8;
  }
  const u16* wsrc[3];
#pragma unroll
  for (int i = 0; i < 3; ++i) {
    int off = (wv * 3 + i) * 1024 + lane * 16;
    int row = off / 192, col = (off - row * 192) >> 1;
    int gc = oct0 * 8 + col;
    if (gc > FF - 8) gc = FF - 8;
    wsrc[i] = p.w1 + (size_t)row * FF + gc;
  }
  const unsigned lbase = __builtin_amdgcn_readfirstlane((unsigned)(size_t)(__attribute__((address_space(3))) char*)smem);
  auto issue = [&](int s) {
    unsigned xd = lbase + (s & (NSTG - 1)) * XSTG + __builtin_amdgcn_readfirstlane(wv) * 4096;
    unsigned wd = lbase + NSTG * XSTG + (s & (NSTG - 1)) * WSTG + __builtin_amdgcn_readfirstlane(wv) * 3072;
#pragma unroll
    for (int i = 0; i < 4; ++i) glds16(xsrc[i] + s * 64, xd + i * 1024);
#pragma unroll
    for (int i = 0; i < 3; ++i) glds16(wsrc[i] + (size_t)s * 64 * FF, wd + i * 1024);
  };
  issue(0); issue(1); issue(2);

  {
    char* sb = smem + K1_SIDE_OFF;
    float kvv[2][8];
#pragma unroll
    for (int rr = 0; rr < 2; ++rr) {
#pragma unroll
      for (int c = 0; c < 8; ++c) {
        const u32x4& sk = skr[c >> 1];
        int b0 = (c & 1) * 4;
        float s0 = bfv(sk, b0 + 0), s1 = bfv(sk, b0 + 1), s2 = bfv(sk, b0 + 2), s3 = bfv(sk, b0 + 3);
        float acc = rbf(bfv(kr[rr + 0], c) * s3);
        acc = rbf(acc + rbf(bfv(kr[rr + 1], c) * s2));
        acc = rbf(acc + rbf(bfv(kr[rr + 2], c) * s1));
        acc = rbf(acc + rbf(bfv(kr[rr + 3], c) * s0));
        kvv[rr][c] = rbf(acc * bfv(vr[rr], c));
      }
    }
#pragma unroll
    for (int rr = 0; rr < 2; ++rr) {
      u32x4 o;
#pragma unroll
      for (int c = 0; c < 4; ++c) o[c] = pk2(kvv[rr][2 * c], kvv[rr][2 * c + 1]);
      *(u32x4*)(p.kvN + (size_t)(t0 + 2 * rg + rr) * DM + cb * 64 + so * 8) = o;
    }
#pragma unroll
    for (int c = 0; c < 8; ++c)
      *(unsigned*)(sb + (so * 8 + c) * 144 + rg * 4) = pk2(kvv[0][c], kvv[1][c]);
    __syncthreads();
    {
      int ch = tid >> 2, qt = tid & 3;
      u32x4 a = *(const u32x4*)(sb + ch * 144 + qt * 32);
      u32x4 b = *(const u32x4*)(sb + ch * 144 + qt * 32 + 16);
      u16* dst = p.uT + (size_t)(cb * 64 + ch) * SEQ + t0 + qt * 16;
      *(u32x4*)dst = a;
      *(u32x4*)(dst + 8) = b;
    }
#pragma unroll
    for (int i = 0; i < 2; ++i) {
      float s = 0.f;
#pragma unroll
      for (int c = 0; c < 8; ++c) s += rbf(bfv(xg[i], c) * bfv(gwr, c));
#pragma unroll
      for (int m = 32; m >= 1; m >>= 1) s += __shfl_xor(s, m);
      s = rbf(s);
      float gg = rbf(1.0f / (1.0f + __expf(-s)));
      if (lane == 0) p.g[8 * w + 2 * wv + i] = gg;
    }
    {
      unsigned e[3][4];
      const int n = lane & 31, hh = lane >> 5;
#pragma unroll
      for (int j = 0; j < 3; ++j) {
        int blk = w * 4 + wv + j * 1024;
        int bc = blk < 16 * NOCTP ? blk : 0;
        int nb = bc / NOCTP, o = bc - nb * NOCTP;
        int oc = o < NOCT ? o : 0;
        const u16* src = p.w2 + (size_t)(oc * 8 + hh * 4) * DM + nb * 32 + n;
        e[j][0] = src[0]; e[j][1] = src[DM]; e[j][2] = src[2 * DM]; e[j][3] = src[3 * DM];
      }
#pragma unroll
      for (int j = 0; j < 3; ++j) {
        int blk = w * 4 + wv + j * 1024;
        if (blk < 16 * NOCTP) {
          int nb = blk / NOCTP, o = blk - nb * NOCTP;
          u32x2 val = {e[j][0] | (e[j][1] << 16), e[j][2] | (e[j][3] << 16)};
          if (o >= NOCT) val = (u32x2){0u, 0u};
          *(u32x2*)(p.w2F + (size_t)blk * 256 + n * 8 + hh * 4) = val;
        }
      }
    }
    if (wv == 0 && lane < 32) {
      int R = w >> 2, o = NOCT + (w & 3);
      *(u32x4*)(p.actF + ((size_t)(R * NOCTP + o) * 32 + lane) * 8) = (u32x4){0u, 0u, 0u, 0u};
    }
    if (tid == 0) p.flags[w * 16] = 0u;
  }

  const int r = lane & 31, h = lane >> 5;
  f32x16 acc[3];
#pragma unroll
  for (int c = 0; c < 3; ++c)
#pragma unroll
    for (int i = 0; i < 16; ++i) acc[c][i] = 0.f;

  const int xrow = wv * 32 + r;
  const int xsw = (xrow >> 1) & 7;
  const int g16 = lane >> 4, q4 = (lane & 15) >> 2, pp = lane & 3;
  const int trrow = 8 * (g16 >> 1) + q4, trcol = 16 * (g16 & 1) + 4 * pp;

#pragma unroll
  for (int s = 0; s < 8; ++s) {
    if (s <= 4) asm volatile("s_waitcnt vmcnt(21)" ::: "memory");
    else if (s == 5) asm volatile("s_waitcnt vmcnt(14)" ::: "memory");
    else if (s == 6) asm volatile("s_waitcnt vmcnt(7)" ::: "memory");
    else asm volatile("s_waitcnt vmcnt(0)" ::: "memory");
    __builtin_amdgcn_s_barrier();
    if (s + 3 < 8) issue(s + 3);
    const char* xb = xs + (s & (NSTG - 1)) * XSTG + xrow * 128;
    const char* wb = ws + (s & (NSTG - 1)) * WSTG;
#pragma unroll
    for (int ks = 0; ks < 4; ++ks) {
      bf16x8 xf = *(const bf16x8*)(xb + (((2 * ks + h) ^ xsw) << 4));
#pragma unroll
      for (int ct = 0; ct < 3; ++ct) {
        const char* a0 = wb + (16 * ks + trrow) * 192 + (32 * ct + trcol) * 2;
        s16x4 lo = __builtin_amdgcn_ds_read_tr16_b64_v4i16((__attribute__((address_space(3))) s16x4*)a0);
        s16x4 hi = __builtin_amdgcn_ds_read_tr16_b64_v4i16((__attribute__((address_space(3))) s16x4*)(a0 + 4 * 192));
        bf16x8 wf = __builtin_shufflevector(lo, hi, 0, 1, 2, 3, 4, 5, 6, 7);
        acc[ct] = __builtin_amdgcn_mfma_f32_32x32x16_bf16(wf, xf, acc[ct], 0, 0, 0);
      }
    }
  }

  const int R = rb * 4 + wv;
#pragma unroll
  for (int ct = 0; ct < 3; ++ct) {
#pragma unroll
    for (int gq = 0; gq < 4; ++gq) {
      int ol = 4 * ct + gq;
      if (ol < noct) {
        int o = oct0 + ol;
        int f = o * 8 + 4 * h;
        u32x2 bb = *(const u32x2*)(p.b1 + f);
        float bv[4] = {bflo(bb[0]), bfhi(bb[0]), bflo(bb[1]), bfhi(bb[1])};
        float res[4];
#pragma unroll
        for (int i = 0; i < 4; ++i) {
          float hv = rbf(rbf(acc[ct][4 * gq + i]) + bv[i]);
          float e = __expf(fminf(hv, 30.f));
          float n = e * (e + 2.f);
          res[i] = hv * n * __builtin_amdgcn_rcpf(n + 2.f);
        }
        u32x2 o2 = {pk2(res[0], res[1]), pk2(res[2], res[3])};
        *(u32x2*)((char*)p.actF + (size_t)(R * NOCTP + o) * 512 + r * 16 + h * 8) = o2;
      }
    }
  }
}

#define CP_STRIDE 4160
#define CONV_U_OFF (2 * 8 * CP_STRIDE)
#define U_CH_STRIDE 7600
#define K2_LDS (CONV_U_OFF + 2 * U_CH_STRIDE)

__device__ __forceinline__ void conv_wg(const Params& p, int c, char* smem) {
  const int tid = threadIdx.x, lane = tid & 63, wv = tid >> 6;
  char* cps = smem;
  char* us = smem + CONV_U_OFF;

  u32x4 uv[2], flo[2], fhi[2];
#pragma unroll
  for (int i = 0; i < 2; ++i) {
    uv[i] = *(const u32x4*)(p.uT + (size_t)(2 * c + i) * SEQ + tid * 8);
    const u16* f = p.ssmk + (size_t)(2 * c + i) * SEQ;
    int i0 = 8 * tid - 8;
    u32x4 t = *(const u32x4*)(f + (i0 < 0 ? 0 : i0));
    flo[i] = i0 < 0 ? (u32x4){0u, 0u, 0u, 0u} : t;
    fhi[i] = *(const u32x4*)(f + 8 * tid);
  }
  for (int z = tid; z < 2 * 155; z += 256) {
    int ch = z / 155, zz = z - ch * 155;
    *(u32x4*)(us + ch * U_CH_STRIDE + zz * 16) = (u32x4){0u, 0u, 0u, 0u};
  }
  if (tid < 48) {
    int cpy = tid / 3, t = tid - cpy * 3;
    *(u32x4*)(cps + cpy * CP_STRIDE + t * 16) = (u32x4){0u, 0u, 0u, 0u};
  }
#pragma unroll
  for (int i = 0; i < 2; ++i)
    *(u32x4*)(us + i * U_CH_STRIDE + ((tid >> 2) + 31) * 80 + (tid & 3) * 16) = uv[i];
#pragma unroll
  for (int i = 0; i < 2; ++i) {
    unsigned wd[8] = {flo[i][0], flo[i][1], flo[i][2], flo[i][3], fhi[i][0], fhi[i][1], fhi[i][2], fhi[i][3]};
    unsigned rp[14];
#pragma unroll
    for (int j = -6; j <= 7; ++j) {
      if (j & 1) { unsigned x = wd[(j + 7) / 2]; rp[j + 6] = (x >> 16) | (x << 16); }
      else { rp[j + 6] = (wd[(j + 8) / 2] & 0xffffu) | (wd[(j + 8) / 2 - 1] & 0xffff0000u); }
    }
#pragma unroll
    for (int cc = 0; cc < 8; ++cc) {
      u32x4 o = {rp[cc + 6], rp[cc - 2 + 6], rp[cc - 4 + 6], rp[cc - 6 + 6]};
      *(u32x4*)(cps + (i * 8 + cc) * CP_STRIDE + (tid + 3) * 16) = o;
    }
  }
  __syncthreads();

  const int ch = wv >> 1, isB = wv & 1;
  const int r = lane & 31, h = lane >> 5;
  const char* ab = cps + (ch * 8 + (r & 7)) * CP_STRIDE + 16 * ((r >> 3) - h + 3);
  const char* ub = us + ch * U_CH_STRIDE + (r + 31) * 80 + 16 * h;

  f32x16 acc0, acc1;
#pragma unroll
  for (int i = 0; i < 16; ++i) { acc0[i] = 0.f; acc1[i] = 0.f; }

  auto run = [&](const char* a, const char* b, int n, f32x16& acc) {
    bf16x8 ca0 = *(const bf16x8*)(a - 32), cb0 = *(const bf16x8*)(b + 32);
    bf16x8 ca1 = *(const bf16x8*)(a), cb1 = *(const bf16x8*)(b);
#pragma unroll 4
    for (int qq = 0; qq < n; ++qq) {
      a += 64; b -= 80;
      bf16x8 na0 = *(const bf16x8*)(a - 32), nb0 = *(const bf16x8*)(b + 32);
      bf16x8 na1 = *(const bf16x8*)(a), nb1 = *(const bf16x8*)(b);
      SCHED_FENCE();
      acc = __builtin_amdgcn_mfma_f32_32x32x16_bf16(ca0, cb0, acc, 0, 0, 0);
      acc = __builtin_amdgcn_mfma_f32_32x32x16_bf16(ca1, cb1, acc, 0, 0, 0);
      SCHED_FENCE();
      ca0 = na0; cb0 = nb0; ca1 = na1; cb1 = nb1;
    }
  };
  if (isB) {
    run(ab, ub, 32, acc0);
    run(ab + 48 * 64, ub + 32 * 80 - 48 * 80, 16, acc1);
  } else {
    run(ab, ub + 32 * 80, 48, acc0);
  }
  __syncthreads();
  float* red = (float*)(smem + ch * 4096);
  if (isB) {
#pragma unroll
    for (int i = 0; i < 16; ++i) red[i * 64 + lane] = acc1[i];
  }
  __syncthreads();
  if (!isB) {
#pragma unroll
    for (int i = 0; i < 16; ++i) acc0[i] += red[i * 64 + lane];
  }
  u16* ost = (u16*)(smem + 8192) + ch * SEQ;
  {
    const int tau = isB ? 0 : 1;
#pragma unroll
    for (int gq = 0; gq < 4; ++gq) {
      u32x2 o = {pk2(acc0[4 * gq], acc0[4 * gq + 1]), pk2(acc0[4 * gq + 2], acc0[4 * gq + 3])};
      *(u32x2*)(ost + 1024 * tau + 32 * r + 8 * gq + 4 * h) = o;
    }
  }
  __syncthreads();
  {
    auto rsrc = __builtin_amdgcn_make_buffer_rsrc((void*)p.yT, 0, DM * SEQ * 2, 0x00020000);
#pragma unroll
    for (int i = 0; i < 2; ++i) {
      int x = tid + i * 256;
      int chh = x >> 8, xx = x & 255;
      u32x4 val = *(const u32x4*)((const u16*)(smem + 8192) + chh * SEQ + xx * 8);
      __builtin_amdgcn_raw_buffer_store_b128(val, rsrc, ((2 * c + chh) * SEQ + xx * 8) * 2, 0, 16);
    }
  }
  asm volatile("s_waitcnt vmcnt(0)" ::: "memory");
  __syncthreads();
  if (tid == 0) __hip_atomic_store(p.flags + c * 16, 1u, __ATOMIC_RELAXED, __HIP_MEMORY_SCOPE_AGENT);
}

#define RED_PITCH 68
__device__ __forceinline__ void gemm2_wg(const Params& p, int gi, char* smem) {
  const int tid = threadIdx.x, lane = tid & 63, wv = tid >> 6;
  const int grp = gi & 7, idx = gi >> 3;
  const int rb = 4 * grp + (idx >> 3);
  const int cb = idx & 7;

  const int erow = tid >> 2, ecol = (tid & 3) * 16;
  const int s = rb * 64 + erow, d0 = cb * 64 + ecol;
  u32x4 qv[2], kvv[2], dv[2];
#pragma unroll
  for (int i = 0; i < 2; ++i) {
    qv[i] = *(const u32x4*)(p.q + (size_t)s * DM + d0 + i * 8);
    kvv[i] = *(const u32x4*)(p.kvN + (size_t)s * DM + d0 + i * 8);
    dv[i] = *(const u32x4*)(p.ssmD + d0 + i * 8);
  }
  float gg = p.g[s];

  f32x16 acc[2][2];
#pragma unroll
  for (int a = 0; a < 2; ++a)
#pragma unroll
    for (int b = 0; b < 2; ++b)
#pragma unroll
      for (int i = 0; i < 16; ++i) acc[a][b][i] = 0.f;

  const char* ap = (const char*)p.actF + ((size_t)(2 * rb) * NOCTP + wv * 44) * 512 + lane * 16;
  const char* bp = (const char*)p.w2F + ((size_t)(2 * cb) * NOCTP + wv * 44) * 512 + lane * 16;
  constexpr int PF = 4;
  bf16x8 fa[PF][2], fb[PF][2];
#pragma unroll
  for (int i = 0; i < PF; ++i) {
    fa[i][0] = *(const bf16x8*)(ap + i * 1024);
    fa[i][1] = *(const bf16x8*)(ap + NOCTP * 512 + i * 1024);
    fb[i][0] = *(const bf16x8*)(bp + i * 1024);
    fb[i][1] = *(const bf16x8*)(bp + NOCTP * 512 + i * 1024);
  }
#pragma unroll
  for (int ks = 0; ks < 22; ++ks) {
    const int i = ks % PF;
    bf16x8 a0 = fa[i][0], a1 = fa[i][1], b0 = fb[i][0], b1 = fb[i][1];
    if (ks + PF < 22) {
      fa[i][0] = *(const bf16x8*)(ap + (ks + PF) * 1024);
      fa[i][1] = *(const bf16x8*)(ap + NOCTP * 512 + (ks + PF) * 1024);
      fb[i][0] = *(const bf16x8*)(bp + (ks + PF) * 1024);
      fb[i][1] = *(const bf16x8*)(bp + NOCTP * 512 + (ks + PF) * 1024);
    }
    SCHED_FENCE();
    acc[0][0] = __builtin_amdgcn_mfma_f32_32x32x16_bf16(a0, b0, acc[0][0], 0, 0, 0);
    acc[0][1] = __builtin_amdgcn_mfma_f32_32x32x16_bf16(a0, b1, acc[0][1], 0, 0, 0);
    acc[1][0] = __builtin_amdgcn_mfma_f32_32x32x16_bf16(a1, b0, acc[1][0], 0, 0, 0);
    acc[1][1] = __builtin_amdgcn_mfma_f32_32x32x16_bf16(a1, b1, acc[1][1], 0, 0, 0);
    SCHED_FENCE();
  }

  float* red = (float*)smem;
  const int r = lane & 31, h = lane >> 5;
#pragma unroll
  for (int a = 0; a < 2; ++a)
#pragma unroll
    for (int b = 0; b < 2; ++b)
#pragma unroll
      for (int i = 0; i < 16; ++i) {
        int m = a * 32 + (i & 3) + 8 * (i >> 2) + 4 * h;
        red[(wv * 64 + m) * RED_PITCH + b * 32 + r] = acc[a][b][i];
      }

  if (wv == 0) {
    const unsigned* fl = p.flags + (cb * 32 + (lane & 31)) * 16;
    for (int spin = 0; spin < (1 << 22); ++spin) {
      unsigned v = __hip_atomic_load(fl, __ATOMIC_RELAXED, __HIP_MEMORY_SCOPE_AGENT);
      if (__all(v != 0u)) break;
      __builtin_amdgcn_s_sleep(2);
    }
  }
  __syncthreads();
  __builtin_amdgcn_fence(__ATOMIC_ACQUIRE, "wavefront");

  u16* ys = (u16*)(smem + 4 * 64 * RED_PITCH * 4);
  {
    auto rsrc = __builtin_amdgcn_make_buffer_rsrc((void*)p.yT, 0, DM * SEQ * 2, 0x00020000);
    int dd = tid >> 2, qt = tid & 3;
    int goff = ((cb * 64 + dd) * SEQ + rb * 64 + qt * 16) * 2;
    u32x4 v0 = __builtin_amdgcn_raw_buffer_load_b128(rsrc, goff, 0, 16);
    u32x4 v1 = __builtin_amdgcn_raw_buffer_load_b128(rsrc, goff + 16, 0, 16);
    *(u32x4*)(ys + dd * 72 + qt * 16) = v0;
    *(u32x4*)(ys + dd * 72 + qt * 16 + 8) = v1;
  }
  __syncthreads();

  float bsum[16];
#pragma unroll
  for (int j = 0; j < 4; ++j) {
    f32x4 t = *(const f32x4*)(red + (0 * 64 + erow) * RED_PITCH + ecol + j * 4);
#pragma unroll
    for (int ww = 1; ww < 4; ++ww) t += *(const f32x4*)(red + (ww * 64 + erow) * RED_PITCH + ecol + j * 4);
#pragma unroll
    for (int i = 0; i < 4; ++i) bsum[j * 4 + i] = t[i];
  }
  const float omg = rbf(1.0f - gg);
  float res[16];
#pragma unroll
  for (int j = 0; j < 16; ++j) {
    float y = bflo((unsigned)ys[(ecol + j) * 72 + erow]);
    float kv = bfv(kvv[j >> 3], j & 7), dd = bfv(dv[j >> 3], j & 7), qq = bfv(qv[j >> 3], j & 7);
    float ysum = rbf(y + rbf(kv * dd));
    float a = rbf(qq * ysum);
    float b = rbf(bsum[j]);
    res[j] = rbf(gg * a) + rbf(omg * b);
  }
  u32x4 o0, o1;
#pragma unroll
  for (int j = 0; j < 4; ++j) { o0[j] = pk2(res[2 * j], res[2 * j + 1]); o1[j] = pk2(res[8 + 2 * j], res[8 + 2 * j + 1]); }
  *(u32x4*)(p.out + (size_t)s * DM + d0) = o0;
  *(u32x4*)(p.out + (size_t)s * DM + d0 + 8) = o1;
}

__global__ __launch_bounds__(256, 2) void k2_kernel(Params p) {
  __shared__ __attribute__((aligned(16))) char smem[K2_LDS];
  if (blockIdx.x < 256) conv_wg(p, blockIdx.x, smem);
  else gemm2_wg(p, blockIdx.x - 256, smem);
}

extern "C" void kernel_launch(void* const* d_in, const int* in_sizes, int n_in,
                              void* d_out, int out_size, void* d_ws, size_t ws_size,
                              hipStream_t stream) {
  Params p;
  p.q = (const u16*)d_in[0];
  p.k = (const u16*)d_in[1];
  p.v = (const u16*)d_in[2];
  p.shk = (const u16*)d_in[3];
  p.ssmk = (const u16*)d_in[4];
  p.ssmD = (const u16*)d_in[5];
  p.w1 = (const u16*)d_in[7];
  p.b1 = (const u16*)d_in[8];
  p.w2 = (const u16*)d_in[9];
  p.gw = (const u16*)d_in[10];
  p.out = (u16*)d_out;
  char* ws = (char*)d_ws;
  size_t off = 0;
  p.uT = (u16*)(ws + off);   off += (size_t)DM * SEQ * 2;
  p.kvN = (u16*)(ws + off);  off += (size_t)DM * SEQ * 2;
  p.yT = (u16*)(ws + off);   off += (size_t)DM * SEQ * 2;
  p.actF = (u16*)(ws + off); off += (size_t)64 * NOCTP * 512;
  p.w2F = (u16*)(ws + off);  off += (size_t)16 * NOCTP * 512;
  p.g = (float*)(ws + off);  off += SEQ * 4;
  p.flags = (unsigned*)(ws + off); off += 256 * 64;
  k1_kernel<<<256, 256, 0, stream>>>(p);
  k2_kernel<<<512, 256, 0, stream>>>(p);
}
```

```cpp
#include <hip/hip_runtime.h>
#include <hip/hip_bf16.h>
#include <stdint.h>

#define STAMP(kid, i)
#define SEQ   2048
#define DM    512
#define FF    1376
#define NOCT  172
#define NOCTP 176

typedef unsigned short u16;
typedef __attribute__((ext_vector_type(8))) short bf16x8;
typedef __attribute__((ext_vector_type(4))) short s16x4;
typedef __attribute__((ext_vector_type(16))) float f32x16;
typedef __attribute__((ext_vector_type(4))) float f32x4;
typedef __attribute__((ext_vector_type(4))) unsigned int u32x4;
typedef __attribute__((ext_vector_type(2))) unsigned int u32x2;

struct Params {
  const u16* q; const u16* k; const u16* v; const u16* shk; const u16* ssmk; const u16* ssmD;
  const u16* w1; const u16* b1; const u16* w2; const u16* gw;
  u16* out;
  u16* uT; u16* kvN; u16* yT; u16* actF; u16* w2F;
  float* g; unsigned* flags;
};

__device__ __forceinline__ unsigned pk2(float a, float b) {
  typedef __attribute__((ext_vector_type(2))) __bf16 bf2;
  typedef __attribute__((ext_vector_type(2))) float f2;
  f2 v = {a, b};
  bf2 r = __builtin_convertvector(v, bf2);
  return __builtin_bit_cast(unsigned, r);
}
__device__ __forceinline__ float bflo(unsigned u) { return __builtin_bit_cast(float, u << 16); }
__device__ __forceinline__ float bfhi(unsigned u) { return __builtin_bit_cast(float, u & 0xffff0000u); }
__device__ __forceinline__ float rbf(float x) { return bflo(pk2(x, x) & 0xffffu); }
__device__ __forceinline__ float bfv(const u32x4& w, int i) {
  unsigned d = w[i >> 1];
  return (i & 1) ? bfhi(d) : bflo(d);
}

#define LDS_PTR(p) ((__attribute__((address_space(3))) void*)(p))

__device__ __forceinline__ void glds16(const void* gsrc, unsigned lds_dst) {
  unsigned keep;
  asm volatile("s_mov_b32 %0, m0\n\ts_mov_b32 m0, %2\n\ts_nop 0\n\tglobal_load_lds_dwordx4 %1, off\n\ts_mov_b32 m0, %0"
               : "=&s"(keep) : "v"(gsrc), "s"(lds_dst) : "memory");
}
#define SCHED_FENCE() __builtin_amdgcn_sched_barrier(0)

#define NSTG 4
#define XSTG 16384
#define WSTG 12288
#define K1_SIDE_OFF (NSTG * (XSTG + WSTG))
#define K1_LDS (K1_SIDE_OFF + 64 * 144)

__global__ __launch_bounds__(256, 1) void k1_kernel(Params p) {
  __shared__ __attribute__((aligned(16))) char smem[K1_LDS];
  const int tid = threadIdx.x, lane = tid & 63, wv = tid >> 6;
  const int w = blockIdx.x;
  const int grp = w & 7, idx = w >> 3;
  const int rb = 2 * grp + (idx >> 4);
  const int jt = idx & 15;
  const int oct0 = jt < 12 ? 11 * jt : 132 + 10 * (jt - 12);
  const int noct = jt < 12 ? 11 : 10;

  char* xs = smem;
  char* ws = smem + NSTG * XSTG;
  STAMP(0, 0);

  const int tb = w >> 3, cb = w & 7;
  const int so = tid & 7, rg = tid >> 3;
  const int t0 = tb * 64;
  u32x4 kr[5], vr[2], skr[4];
#pragma unroll
  for (int i = 0; i < 5; ++i) {
    int row = t0 + 2 * rg - 3 + i;
    int rc = row < 0 ? 0 : row;
    u32x4 t = *(const u32x4*)(p.k + (size_t)rc * DM + cb * 64 + so * 8);
    kr[i] = row < 0 ? (u32x4){0u, 0u, 0u, 0u} : t;
  }
#pragma unroll
  for (int i = 0; i < 2; ++i)
    vr[i] = *(const u32x4*)(p.v + (size_t)(t0 + 2 * rg + i) * DM + cb * 64 + so * 8);
#pragma unroll
  for (int i = 0; i < 4; ++i)
    skr[i] = *(const u32x4*)(p.shk + (size_t)(cb * 64 + so * 8) * 4 + i * 8);
  u32x4 xg[2], gwr;
#pragma unroll
  for (int i = 0; i < 2; ++i)
    xg[i] = *(const u32x4*)(p.q + (size_t)(8 * w + 2 * wv + i) * DM + lane * 8);
  gwr = *(const u32x4*)(p.gw + lane * 8);

  unsigned e[3][4];
  {
    const int n = lane & 31, hh = lane >> 5;
#pragma unroll
    for (int j = 0; j < 3; ++j) {
      int blk = w * 4 + wv + j * 1024;
      int bc = blk < 16 * NOCTP ? blk : 0;
      int nb = bc / NOCTP, o = bc - nb * NOCTP;
      int oc = o < NOCT ? o : 0;
      const u16* src = p.w2 + (size_t)(oc * 8 + hh * 4) * DM + nb * 32 + n;
      e[j][0] = src[0]; e[j][1] = src[DM]; e[j][2] = src[2 * DM]; e[j][3] = src[3 * DM];
    }
  }
  u32x2 b1r[3][4];
#pragma unroll
  for (int ct = 0; ct < 3; ++ct)
#pragma unroll
    for (int gq = 0; gq < 4; ++gq) {
      int o = oct0 + 4 * ct + gq; if (o > NOCT - 1) o = NOCT - 1;
      b1r[ct][gq] = *(const u32x2*)(p.b1 + o * 8 + 4 * (lane >> 5));
    }
  const u16* xsrc[4];
#pragma unroll
  for (int i = 0; i < 4; ++i) {
    int off = (wv * 4 + i) * 1024 + lane * 16;
    int row = off >> 7, pc = (off & 127) >> 4;
    int lc = pc ^ ((row >> 1) & 7);
    xsrc[i] = p.q + (size_t)(rb * 128 + row) * DM + lc * 8;
  }
  const u16* wsrc[3];
#pragma unroll
  for (int i = 0; i < 3; ++i) {
    int off = (wv * 3 + i) * 1024 + lane * 16;
    int row = off / 192, col = (off - row * 192) >> 1;
    int gc = oct0 * 8 + col;
    if (gc > FF - 8) gc = FF - 8;
    wsrc[i] = p.w1 + (size_t)row * FF + gc;
  }
  const unsigned lbase = __builtin_amdgcn_readfirstlane((unsigned)(size_t)(__attribute__((address_space(3))) char*)smem);
  auto issue = [&](int s) {
    unsigned xd = lbase + (s & (NSTG - 1)) * XSTG + __builtin_amdgcn_readfirstlane(wv) * 4096;
    unsigned wd = lbase + NSTG * XSTG + (s & (NSTG - 1)) * WSTG + __builtin_amdgcn_readfirstlane(wv) * 3072;
#pragma unroll
    for (int i = 0; i < 4; ++i) glds16(xsrc[i] + s * 64, xd + i * 1024);
#pragma unroll
    for (int i = 0; i < 3; ++i) glds16(wsrc[i] + (size_t)s * 64 * FF, wd + i * 1024);
  };
  issue(0); issue(1); issue(2);

  STAMP(0, 1);
  const int r = lane & 31, h = lane >> 5;
  f32x16 acc[3];
#pragma unroll
  for (int c = 0; c < 3; ++c)
#pragma unroll
    for (int i = 0; i < 16; ++i) acc[c][i] = 0.f;

  const int xrow = wv * 32 + r;
  const int xsw = (xrow >> 1) & 7;
  const int g16 = lane >> 4, q4 = (lane & 15) >> 2, pp = lane & 3;
  const int trrow = 8 * (g16 >> 1) + q4, trcol = 16 * (g16 & 1) + 4 * pp;

#pragma unroll
  for (int s = 0; s < 8; ++s) {
    if (s <= 4) asm volatile("s_waitcnt vmcnt(21)" ::: "memory");
    else if (s == 5) asm volatile("s_waitcnt vmcnt(14)" ::: "memory");
    else if (s == 6) asm volatile("s_waitcnt vmcnt(7)" ::: "memory");
    else asm volatile("s_waitcnt vmcnt(0)" ::: "memory");
    __builtin_amdgcn_s_barrier();
    if (s == 0) STAMP(0, 2);
    if (s + 3 < 8) issue(s + 3);
    const char* xb = xs + (s & (NSTG - 1)) * XSTG + xrow * 128;
    const char* wb = ws + (s & (NSTG - 1)) * WSTG;
#pragma unroll
    for (int ks = 0; ks < 4; ++ks) {
      bf16x8 xf = *(const bf16x8*)(xb + (((2 * ks + h) ^ xsw) << 4));
#pragma unroll
      for (int ct = 0; ct < 3; ++ct) {
        const char* a0 = wb + (16 * ks + trrow) * 192 + (32 * ct + trcol) * 2;
        s16x4 lo = __builtin_amdgcn_ds_read_tr16_b64_v4i16((__attribute__((address_space(3))) s16x4*)a0);
        s16x4 hi = __builtin_amdgcn_ds_read_tr16_b64_v4i16((__attribute__((address_space(3))) s16x4*)(a0 + 4 * 192));
        bf16x8 wf = __builtin_shufflevector(lo, hi, 0, 1, 2, 3, 4, 5, 6, 7);
        acc[ct] = __builtin_amdgcn_mfma_f32_32x32x16_bf16(wf, xf, acc[ct], 0, 0, 0);
      }
    }
  }

  STAMP(0, 3);
  const int R = rb * 4 + wv;
#pragma unroll
  for (int ct = 0; ct < 3; ++ct) {
#pragma unroll
    for (int gq = 0; gq < 4; ++gq) {
      int ol = 4 * ct + gq;
      if (ol < noct) {
        int o = oct0 + ol;
        u32x2 bb = b1r[ct][gq];
        float bv[4] = {bflo(bb[0]), bfhi(bb[0]), bflo(bb[1]), bfhi(bb[1])};
        float res[4];
#pragma unroll
        for (int i = 0; i < 4; ++i) {
          float hv = rbf(rbf(acc[ct][4 * gq + i]) + bv[i]);
          float e = __expf(fminf(hv, 30.f));
          float n = e * (e + 2.f);
          res[i] = hv * n * __builtin_amdgcn_rcpf(n + 2.f);
        }
        u32x2 o2 = {pk2(res[0], res[1]), pk2(res[2], res[3])};
        *(u32x2*)((char*)p.actF + (size_t)(R * NOCTP + o) * 512 + r * 16 + h * 8) = o2;
      }
    }
  }
  {
    char* sb = smem + K1_SIDE_OFF;
    float kvv[2][8];
#pragma unroll
    for (int rr = 0; rr < 2; ++rr) {
#pragma unroll
      for (int c = 0; c < 8; ++c) {
        const u32x4& sk = skr[c >> 1];
        int b0 = (c & 1) * 4;
        float s0 = bfv(sk, b0 + 0), s1 = bfv(sk, b0 + 1), s2 = bfv(sk, b0 + 2), s3 = bfv(sk, b0 + 3);
        float acc = rbf(bfv(kr[rr + 0], c) * s3);
        acc = rbf(acc + rbf(bfv(kr[rr + 1], c) * s2));
        acc = rbf(acc + rbf(bfv(kr[rr + 2], c) * s1));
        acc = rbf(acc + rbf(bfv(kr[rr + 3], c) * s0));
        kvv[rr][c] = rbf(acc * bfv(vr[rr], c));
      }
    }
#pragma unroll
    for (int rr = 0; rr < 2; ++rr) {
      u32x4 o;
#pragma unroll
      for (int c = 0; c < 4; ++c) o[c] = pk2(kvv[rr][2 * c], kvv[rr][2 * c + 1]);
      *(u32x4*)(p.kvN + (size_t)(t0 + 2 * rg + rr) * DM + cb * 64 + so * 8) = o;
    }
#pragma unroll
    for (int c = 0; c < 8; ++c)
      *(unsigned*)(sb + (so * 8 + c) * 144 + rg * 4) = pk2(kvv[0][c], kvv[1][c]);
    __syncthreads();
    {
      int ch = tid >> 2, qt = tid & 3;
      u32x4 a = *(const u32x4*)(sb + ch * 144 + qt * 32);
      u32x4 b = *(const u32x4*)(sb + ch * 144 + qt * 32 + 16);
      u16* dst = p.uT + (size_t)(cb * 64 + ch) * SEQ + t0 + qt * 16;
      *(u32x4*)dst = a;
      *(u32x4*)(dst + 8) = b;
    }
#pragma unroll
    for (int i = 0; i < 2; ++i) {
      float s = 0.f;
#pragma unroll
      for (int c = 0; c < 8; ++c) s += rbf(bfv(xg[i], c) * bfv(gwr, c));
#pragma unroll
      for (int m = 32; m >= 1; m >>= 1) s += __shfl_xor(s, m);
      s = rbf(s);
      float gg = rbf(1.0f / (1.0f + __expf(-s)));
      if (lane == 0) p.g[8 * w + 2 * wv + i] = gg;
    }
    {
      const int n = lane & 31, hh = lane >> 5;
#pragma unroll
      for (int j = 0; j < 3; ++j) {
        int blk = w * 4 + wv + j * 1024;
        if (blk < 16 * NOCTP) {
          int nb = blk / NOCTP, o = blk - nb * NOCTP;
          u32x2 val = {e[j][0] | (e[j][1] << 16), e[j][2] | (e[j][3] << 16)};
          if (o >= NOCT) val = (u32x2){0u, 0u};
          *(u32x2*)(p.w2F + (size_t)blk * 256 + n * 8 + hh * 4) = val;
        }
      }
    }
    if (wv == 0 && lane < 32) {
      int R = w >> 2, o = NOCT + (w & 3);
      *(u32x4*)(p.actF + ((size_t)(R * NOCTP + o) * 32 + lane) * 8) = (u32x4){0u, 0u, 0u, 0u};
    }
    if (tid == 0) p.flags[w * 16] = 0u;
  }

  STAMP(0, 4);
}

#define CP_STRIDE 4160
#define CONV_U_OFF (2 * 8 * CP_STRIDE)
#define U_CH_STRIDE 7600
#define K2_LDS (CONV_U_OFF + 2 * U_CH_STRIDE)

__device__ __forceinline__ void conv_wg(const Params& p, int c, char* smem) {
  const int tid = threadIdx.x, lane = tid & 63, wv = tid >> 6;
  char* cps = smem;
  char* us = smem + CONV_U_OFF;
  STAMP(1, 0);

  u32x4 uv[2], flo[2], fhi[2];
#pragma unroll
  for (int i = 0; i < 2; ++i) {
    uv[i] = *(const u32x4*)(p.uT + (size_t)(2 * c + i) * SEQ + tid * 8);
    const u16* f = p.ssmk + (size_t)(2 * c + i) * SEQ;
    int i0 = 8 * tid - 8;
    u32x4 t = *(const u32x4*)(f + (i0 < 0 ? 0 : i0));
    flo[i] = i0 < 0 ? (u32x4){0u, 0u, 0u, 0u} : t;
    fhi[i] = *(const u32x4*)(f + 8 * tid);
  }
  for (int z = tid; z < 2 * 155; z += 256) {
    int ch = z / 155, zz = z - ch * 155;
    *(u32x4*)(us + ch * U_CH_STRIDE + zz * 16) = (u32x4){0u, 0u, 0u, 0u};
  }
  if (tid < 48) {
    int cpy = tid / 3, t = tid - cpy * 3;
    *(u32x4*)(cps + cpy * CP_STRIDE + t * 16) = (u32x4){0u, 0u, 0u, 0u};
  }
#pragma unroll
  for (int i = 0; i < 2; ++i)
    *(u32x4*)(us + i * U_CH_STRIDE + ((tid >> 2) + 31) * 80 + (tid & 3) * 16) = uv[i];
#pragma unroll
  for (int i = 0; i < 2; ++i) {
    unsigned wd[8] = {flo[i][0], flo[i][1], flo[i][2], flo[i][3], fhi[i][0], fhi[i][1], fhi[i][2], fhi[i][3]};
    unsigned rp[14];
#pragma unroll
    for (int j = -6; j <= 7; ++j) {
      if (j & 1) { unsigned x = wd[(j + 7) / 2]; rp[j + 6] = (x >> 16) | (x << 16); }
      else { rp[j + 6] = (wd[(j + 8) / 2] & 0xffffu) | (wd[(j + 8) / 2 - 1] & 0xffff0000u); }
    }
#pragma unroll
    for (int cc = 0; cc < 8; ++cc) {
      u32x4 o = {rp[cc + 6], rp[cc - 2 + 6], rp[cc - 4 + 6], rp[cc - 6 + 6]};
      *(u32x4*)(cps + (i * 8 + cc) * CP_STRIDE + (tid + 3) * 16) = o;
    }
  }
  __syncthreads();

  STAMP(1, 1);
  const int ch = wv >> 1, isB = wv & 1;
  const int r = lane & 31, h = lane >> 5;
  const char* ab = cps + (ch * 8 + (r & 7)) * CP_STRIDE + 16 * ((r >> 3) - h + 3);
  const char* ub = us + ch * U_CH_STRIDE + (r + 31) * 80 + 16 * h;

  f32x16 acc0, acc1;
#pragma unroll
  for (int i = 0; i < 16; ++i) { acc0[i] = 0.f; acc1[i] = 0.f; }

  auto run = [&](const char* a, const char* b, int n, f32x16& acc) {
    bf16x8 ca0 = *(const bf16x8*)(a - 32), cb0 = *(const bf16x8*)(b + 32);
    bf16x8 ca1 = *(const bf16x8*)(a), cb1 = *(const bf16x8*)(b);
#pragma unroll 4
    for (int qq = 0; qq < n; ++qq) {
      a += 64; b -= 80;
      bf16x8 na0 = *(const bf16x8*)(a - 32), nb0 = *(const bf16x8*)(b + 32);
      bf16x8 na1 = *(const bf16x8*)(a), nb1 = *(const bf16x8*)(b);
      SCHED_FENCE();
      acc = __builtin_amdgcn_mfma_f32_32x32x16_bf16(ca0, cb0, acc, 0, 0, 0);
      acc = __builtin_amdgcn_mfma_f32_32x32x16_bf16(ca1, cb1, acc, 0, 0, 0);
      SCHED_FENCE();
      ca0 = na0; cb0 = nb0; ca1 = na1; cb1 = nb1;
    }
  };
  if (isB) {
    run(ab, ub, 32, acc0);
    run(ab + 48 * 64, ub + 32 * 80 - 48 * 80, 16, acc1);
  } else {
    run(ab, ub + 32 * 80, 48, acc0);
  }
  __syncthreads();
  STAMP(1, 2);
  float* red = (float*)(smem + ch * 4096);
  if (isB) {
#pragma unroll
    for (int i = 0; i < 16; ++i) red[i * 64 + lane] = acc1[i];
  }
  __syncthreads();
  if (!isB) {
#pragma unroll
    for (int i = 0; i < 16; ++i) acc0[i] += red[i * 64 + lane];
  }
  u16* ost = (u16*)(smem + 8192) + ch * SEQ;
  {
    const int tau = isB ? 0 : 1;
#pragma unroll
    for (int gq = 0; gq < 4; ++gq) {
      u32x2 o = {pk2(acc0[4 * gq], acc0[4 * gq + 1]), pk2(acc0[4 * gq + 2], acc0[4 * gq + 3])};
      *(u32x2*)(ost + 1024 * tau + 32 * r + 8 * gq + 4 * h) = o;
    }
  }
  __syncthreads();
  {
    auto rsrc = __builtin_amdgcn_make_buffer_rsrc((void*)p.yT, 0, DM * SEQ * 2, 0x00020000);
#pragma unroll
    for (int i = 0; i < 2; ++i) {
      int x = tid + i * 256;
      int chh = x >> 8, xx = x & 255;
      u32x4 val = *(const u32x4*)((const u16*)(smem + 8192) + chh * SEQ + xx * 8);
      __builtin_amdgcn_raw_buffer_store_b128(val, rsrc, ((2 * c + chh) * SEQ + xx * 8) * 2, 0, 16);
    }
  }
  asm volatile("s_waitcnt vmcnt(0)" ::: "memory");
  __syncthreads();
  if (tid == 0) __hip_atomic_store(p.flags + c * 16, 1u, __ATOMIC_RELAXED, __HIP_MEMORY_SCOPE_AGENT);
  STAMP(1, 3);
}

#define RED_PITCH 68
__device__ __forceinline__ void gemm2_wg(const Params& p, int gi, char* smem) {
  const int tid = threadIdx.x, lane = tid & 63, wv = tid >> 6;
  const int grp = gi & 7, idx = gi >> 3;
  const int rb = 4 * grp + (idx >> 3);
  const int cb = idx & 7;

  STAMP(1, 0);
  const int erow = tid >> 2, ecol = (tid & 3) * 16;
  const int s = rb * 64 + erow, d0 = cb * 64 + ecol;
  u32x4 qv[2], kvv[2], dv[2];
#pragma unroll
  for (int i = 0; i < 2; ++i) {
    qv[i] = *(const u32x4*)(p.q + (size_t)s * DM + d0 + i * 8);
    kvv[i] = *(const u32x4*)(p.kvN + (size_t)s * DM + d0 + i * 8);
    dv[i] = *(const u32x4*)(p.ssmD + d0 + i * 8);
  }
  float gg = p.g[s];

  f32x16 acc[2][2];
#pragma unroll
  for (int a = 0; a < 2; ++a)
#pragma unroll
    for (int b = 0; b < 2; ++b)
#pragma unroll
      for (int i = 0; i < 16; ++i) acc[a][b][i] = 0.f;

  const char* ap = (const char*)p.actF + ((size_t)(2 * rb) * NOCTP + wv * 44) * 512 + lane * 16;
  const char* bp = (const char*)p.w2F + ((size_t)(2 * cb) * NOCTP + wv * 44) * 512 + lane * 16;
  constexpr int PF = 8;
  bf16x8 fa[PF][2], fb[PF][2];
#pragma unroll
  for (int i = 0; i < PF; ++i) {
    fa[i][0] = *(const bf16x8*)(ap + i * 1024);
    fa[i][1] = *(const bf16x8*)(ap + NOCTP * 512 + i * 1024);
    fb[i][0] = *(const bf16x8*)(bp + i * 1024);
    fb[i][1] = *(const bf16x8*)(bp + NOCTP * 512 + i * 1024);
  }
#pragma unroll
  for (int ks = 0; ks < 22; ++ks) {
    const int i = ks % PF;
    bf16x8 a0 = fa[i][0], a1 = fa[i][1], b0 = fb[i][0], b1 = fb[i][1];
    if (ks + PF < 22) {
      fa[i][0] = *(const bf16x8*)(ap + (ks + PF) * 1024);
      fa[i][1] = *(const bf16x8*)(ap + NOCTP * 512 + (ks + PF) * 1024);
      fb[i][0] = *(const bf16x8*)(bp + (ks + PF) * 1024);
      fb[i][1] = *(const bf16x8*)(bp + NOCTP * 512 + (ks + PF) * 1024);
    }
    SCHED_FENCE();
    acc[0][0] = __builtin_amdgcn_mfma_f32_32x32x16_bf16(a0, b0, acc[0][0], 0, 0, 0);
    acc[0][1] = __builtin_amdgcn_mfma_f32_32x32x16_bf16(a0, b1, acc[0][1], 0, 0, 0);
    acc[1][0] = __builtin_amdgcn_mfma_f32_32x32x16_bf16(a1, b0, acc[1][0], 0, 0, 0);
    acc[1][1] = __builtin_amdgcn_mfma_f32_32x32x16_bf16(a1, b1, acc[1][1], 0, 0, 0);
    SCHED_FENCE();
  }

  STAMP(1, 1);
  float* red = (float*)smem;
  const int r = lane & 31, h = lane >> 5;
#pragma unroll
  for (int a = 0; a < 2; ++a)
#pragma unroll
    for (int b = 0; b < 2; ++b)
#pragma unroll
      for (int i = 0; i < 16; ++i) {
        int m = a * 32 + (i & 3) + 8 * (i >> 2) + 4 * h;
        red[(wv * 64 + m) * RED_PITCH + b * 32 + r] = acc[a][b][i];
      }

  if (wv == 0) {
    const unsigned* fl = p.flags + (cb * 32 + (lane & 31)) * 16;
    for (int spin = 0; spin < (1 << 22); ++spin) {
      unsigned v = __hip_atomic_load(fl, __ATOMIC_RELAXED, __HIP_MEMORY_SCOPE_AGENT);
      if (__all(v != 0u)) break;
      __builtin_amdgcn_s_sleep(2);
    }
  }
  __syncthreads();
  __builtin_amdgcn_fence(__ATOMIC_ACQUIRE, "wavefront");
  STAMP(1, 2);

  u16* ys = (u16*)(smem + 4 * 64 * RED_PITCH * 4);
  {
    auto rsrc = __builtin_amdgcn_make_buffer_rsrc((void*)p.yT, 0, DM * SEQ * 2, 0x00020000);
    int dd = tid >> 2, qt = tid & 3;
    int goff = ((cb * 64 + dd) * SEQ + rb * 64 + qt * 16) * 2;
    u32x4 v0 = __builtin_amdgcn_raw_buffer_load_b128(rsrc, goff, 0, 16);
    u32x4 v1 = __builtin_amdgcn_raw_buffer_load_b128(rsrc, goff + 16, 0, 16);
    *(u32x4*)(ys + dd * 72 + qt * 16) = v0;
    *(u32x4*)(ys + dd * 72 + qt * 16 + 8) = v1;
  }
  __syncthreads();

  float bsum[16];
#pragma unroll
  for (int j = 0; j < 4; ++j) {
    f32x4 t = *(const f32x4*)(red + (0 * 64 + erow) * RED_PITCH + ecol + j * 4);
#pragma unroll
    for (int ww = 1; ww < 4; ++ww) t += *(const f32x4*)(red + (ww * 64 + erow) * RED_PITCH + ecol + j * 4);
#pragma unroll
    for (int i = 0; i < 4; ++i) bsum[j * 4 + i] = t[i];
  }
  const float omg = rbf(1.0f - gg);
  float res[16];
#pragma unroll
  for (int j = 0; j < 16; ++j) {
    float y = bflo((unsigned)ys[(ecol + j) * 72 + erow]);
    float kv = bfv(kvv[j >> 3], j & 7), dd = bfv(dv[j >> 3], j & 7), qq = bfv(qv[j >> 3], j & 7);
    float ysum = rbf(y + rbf(kv * dd));
    float a = rbf(qq * ysum);
    float b = rbf(bsum[j]);
    res[j] = rbf(gg * a) + rbf(omg * b);
  }
  u32x4 o0, o1;
#pragma unroll
  for (int j = 0; j < 4; ++j) { o0[j] = pk2(res[2 * j], res[2 * j + 1]); o1[j] = pk2(res[8 + 2 * j], res[8 + 2 * j + 1]); }
  *(u32x4*)(p.out + (size_t)s * DM + d0) = o0;
  *(u32x4*)(p.out + (size_t)s * DM + d0 + 8) = o1;
  STAMP(1, 3);
}

__global__ __launch_bounds__(256, 2) void k2_kernel(Params p) {
  __shared__ __attribute__((aligned(16))) char smem[K2_LDS];
  if (blockIdx.x < 256) conv_wg(p, blockIdx.x, smem);
  else gemm2_wg(p, blockIdx.x - 256, smem);
}

extern "C" void kernel_launch(void* const* d_in, const int* in_sizes, int n_in,
                              void* d_out, int out_size, void* d_ws, size_t ws_size,
                              hipStream_t stream) {
  Params p;
  p.q = (const u16*)d_in[0];
  p.k = (const u16*)d_in[1];
  p.v = (const u16*)d_in[2];
  p.shk = (const u16*)d_in[3];
  p.ssmk = (const u16*)d_in[4];
  p.ssmD = (const u16*)d_in[5];
  p.w1 = (const u16*)d_in[7];
  p.b1 = (const u16*)d_in[8];
  p.w2 = (const u16*)d_in[9];
  p.gw = (const u16*)d_in[10];
  p.out = (u16*)d_out;
  char* ws = (char*)d_ws;
  size_t off = 0;
  p.uT = (u16*)(ws + off);   off += (size_t)DM * SEQ * 2;
  p.kvN = (u16*)(ws + off);  off += (size_t)DM * SEQ * 2;
  p.yT = (u16*)(ws + off);   off += (size_t)DM * SEQ * 2;
  p.actF = (u16*)(ws + off); off += (size_t)64 * NOCTP * 512;
  p.w2F = (u16*)(ws + off);  off += (size_t)16 * NOCTP * 512;
  p.g = (float*)(ws + off);  off += SEQ * 4;
  p.flags = (unsigned*)(ws + off); off += 256 * 64;
  k1_kernel<<<256, 256, 0, stream>>>(p);
  k2_kernel<<<512, 256, 0, stream>>>(p);
}
```

```cpp
#include <hip/hip_runtime.h>
#include <hip/hip_bf16.h>
#include <stdint.h>

#define STAMP(kid, i)
#define SEQ   2048
#define DM    512
#define FF    1376
#define NOCT  172
#define NOCTP 176

typedef unsigned short u16;
typedef __attribute__((ext_vector_type(8))) short bf16x8;
typedef __attribute__((ext_vector_type(4))) short s16x4;
typedef __attribute__((ext_vector_type(16))) float f32x16;
typedef __attribute__((ext_vector_type(4))) float f32x4;
typedef __attribute__((ext_vector_type(4))) unsigned int u32x4;
typedef __attribute__((ext_vector_type(2))) unsigned int u32x2;

struct Params {
  const u16* q; const u16* k; const u16* v; const u16* shk; const u16* ssmk; const u16* ssmD;
  const u16* w1; const u16* b1; const u16* w2; const u16* gw;
  u16* out;
  u16* uT; u16* kvN; u16* yT; u16* actF; u16* w2F;
  float* g; unsigned* flags;
};

__device__ __forceinline__ unsigned pk2(float a, float b) {
  typedef __attribute__((ext_vector_type(2))) __bf16 bf2;
  typedef __attribute__((ext_vector_type(2))) float f2;
  f2 v = {a, b};
  bf2 r = __builtin_convertvector(v, bf2);
  return __builtin_bit_cast(unsigned, r);
}
__device__ __forceinline__ float bflo(unsigned u) { return __builtin_bit_cast(float, u << 16); }
__device__ __forceinline__ float bfhi(unsigned u) { return __builtin_bit_cast(float, u & 0xffff0000u); }
__device__ __forceinline__ float rbf(float x) { return bflo(pk2(x, x) & 0xffffu); }
__device__ __forceinline__ float bfv(const u32x4& w, int i) {
  unsigned d = w[i >> 1];
  return (i & 1) ? bfhi(d) : bflo(d);
}

#define LDS_PTR(p) ((__attribute__((address_space(3))) void*)(p))

__device__ __forceinline__ void glds16(const void* gsrc, unsigned lds_dst) {
  unsigned keep;
  asm volatile("s_mov_b32 %0, m0\n\ts_mov_b32 m0, %2\n\ts_nop 0\n\tglobal_load_lds_dwordx4 %1, off\n\ts_mov_b32 m0, %0"
               : "=&s"(keep) : "v"(gsrc), "s"(lds_dst) : "memory");
}
#define SCHED_FENCE() __builtin_amdgcn_sched_barrier(0)

#define NSTG 4
#define XSTG 16384
#define WSTG 12288
#define K1_SIDE_OFF (NSTG * (XSTG + WSTG))
#define K1_LDS (K1_SIDE_OFF + 4 * 64 * 48)

__device__ __forceinline__ void k1_side(const Params& p, int w, int tid, char* sb) {
  const int lane = tid & 63, wv = tid >> 6;
  const int tb = w >> 3, cb = w & 7;
  const int so = tid & 7, rg = tid >> 3;
  const int t0 = tb * 64;
  u32x4 kr[5], vr[2], skr[4];
#pragma unroll
  for (int i = 0; i < 5; ++i) {
    int row = t0 + 2 * rg - 3 + i;
    int rc = row < 0 ? 0 : row;
    u32x4 t = *(const u32x4*)(p.k + (size_t)rc * DM + cb * 64 + so * 8);
    kr[i] = row < 0 ? (u32x4){0u, 0u, 0u, 0u} : t;
  }
#pragma unroll
  for (int i = 0; i < 2; ++i)
    vr[i] = *(const u32x4*)(p.v + (size_t)(t0 + 2 * rg + i) * DM + cb * 64 + so * 8);
#pragma unroll
  for (int i = 0; i < 4; ++i)
    skr[i] = *(const u32x4*)(p.shk + (size_t)(cb * 64 + so * 8) * 4 + i * 8);
  u32x4 xg[2], gwr;
#pragma unroll
  for (int i = 0; i < 2; ++i)
    xg[i] = *(const u32x4*)(p.q + (size_t)(8 * w + 2 * wv + i) * DM + lane * 8);
  gwr = *(const u32x4*)(p.gw + lane * 8);
  unsigned e[3][4];
  const int n = lane & 31, hh = lane >> 5;
#pragma unroll
  for (int j = 0; j < 3; ++j) {
    int blk = w * 4 + wv + j * 1024;
    int bc = blk < 16 * NOCTP ? blk : 0;
    int nb = bc / NOCTP, o = bc - nb * NOCTP;
    int oc = o < NOCT ? o : 0;
    const u16* src = p.w2 + (size_t)(oc * 8 + hh * 4) * DM + nb * 32 + n;
    e[j][0] = src[0]; e[j][1] = src[DM]; e[j][2] = src[2 * DM]; e[j][3] = src[3 * DM];
  }
  float kvv[2][8];
#pragma unroll
  for (int rr = 0; rr < 2; ++rr) {
#pragma unroll
    for (int c = 0; c < 8; ++c) {
      const u32x4& sk = skr[c >> 1];
      int b0 = (c & 1) * 4;
      float s0 = bfv(sk, b0 + 0), s1 = bfv(sk, b0 + 1), s2 = bfv(sk, b0 + 2), s3 = bfv(sk, b0 + 3);
      float acc = rbf(bfv(kr[rr + 0], c) * s3);
      acc = rbf(acc + rbf(bfv(kr[rr + 1], c) * s2));
      acc = rbf(acc + rbf(bfv(kr[rr + 2], c) * s1));
      acc = rbf(acc + rbf(bfv(kr[rr + 3], c) * s0));
      kvv[rr][c] = rbf(acc * bfv(vr[rr], c));
    }
  }
#pragma unroll
  for (int rr = 0; rr < 2; ++rr) {
    u32x4 o;
#pragma unroll
    for (int c = 0; c < 4; ++c) o[c] = pk2(kvv[rr][2 * c], kvv[rr][2 * c + 1]);
    *(u32x4*)(p.kvN + (size_t)(t0 + 2 * rg + rr) * DM + cb * 64 + so * 8) = o;
  }
  char* wb = sb + wv * (64 * 48);
#pragma unroll
  for (int c = 0; c < 8; ++c)
    *(unsigned*)(wb + (so * 8 + c) * 48 + (rg & 7) * 4) = pk2(kvv[0][c], kvv[1][c]);
  __builtin_amdgcn_fence(__ATOMIC_SEQ_CST, "wavefront");
  asm volatile("s_waitcnt lgkmcnt(0)" ::: "memory");
  {
    u32x4 a = *(const u32x4*)(wb + lane * 48);
    u32x4 b = *(const u32x4*)(wb + lane * 48 + 16);
    u16* dst = p.uT + (size_t)(cb * 64 + lane) * SEQ + t0 + wv * 16;
    *(u32x4*)dst = a;
    *(u32x4*)(dst + 8) = b;
  }
#pragma unroll
  for (int i = 0; i < 2; ++i) {
    float s = 0.f;
#pragma unroll
    for (int c = 0; c < 8; ++c) s += rbf(bfv(xg[i], c) * bfv(gwr, c));
#pragma unroll
    for (int m = 32; m >= 1; m >>= 1) s += __shfl_xor(s, m);
    s = rbf(s);
    float gg = rbf(1.0f / (1.0f + __expf(-s)));
    if (lane == 0) p.g[8 * w + 2 * wv + i] = gg;
  }
#pragma unroll
  for (int j = 0; j < 3; ++j) {
    int blk = w * 4 + wv + j * 1024;
    if (blk < 16 * NOCTP) {
      int nb = blk / NOCTP, o = blk - nb * NOCTP;
      u32x2 val = {e[j][0] | (e[j][1] << 16), e[j][2] | (e[j][3] << 16)};
      if (o >= NOCT) val = (u32x2){0u, 0u};
      *(u32x2*)(p.w2F + (size_t)blk * 256 + n * 8 + hh * 4) = val;
    }
  }
  if (wv == 0 && lane < 32) {
    int R = w >> 2, o = NOCT + (w & 3);
    *(u32x4*)(p.actF + ((size_t)(R * NOCTP + o) * 32 + lane) * 8) = (u32x4){0u, 0u, 0u, 0u};
  }
  if (tid == 0) p.flags[w * 16] = 0u;
}

__global__ __launch_bounds__(512, 1) void k1_kernel(Params p) {
  __shared__ __attribute__((aligned(16))) char smem[K1_LDS];
  const int tid = threadIdx.x, lane = tid & 63, wv = tid >> 6;
  const int w = blockIdx.x;
  const int grp = w & 7, idx = w >> 3;
  const int rb = 2 * grp + (idx >> 4);
  const int jt = idx & 15;
  const int oct0 = jt < 12 ? 11 * jt : 132 + 10 * (jt - 12);
  const int noct = jt < 12 ? 11 : 10;

  char* xs = smem;
  char* ws = smem + NSTG * XSTG;
  STAMP(0, 0);
  if (__builtin_amdgcn_readfirstlane(wv) >= 4) { k1_side(p, w, tid - 256, smem + K1_SIDE_OFF); return; }

  u32x2 b1r[3][4];
#pragma unroll
  for (int ct = 0; ct < 3; ++ct)
#pragma unroll
    for (int gq = 0; gq < 4; ++gq) {
      int o = oct0 + 4 * ct + gq; if (o > NOCT - 1) o = NOCT - 1;
      b1r[ct][gq] = *(const u32x2*)(p.b1 + o * 8 + 4 * (lane >> 5));
    }
  const u16* xsrc[4];
#pragma unroll
  for (int i = 0; i < 4; ++i) {
    int off = (wv * 4 + i) * 1024 + lane * 16;
    int row = off >> 7, pc = (off & 127) >> 4;
    int lc = pc ^ ((row >> 1) & 7);
    xsrc[i] = p.q + (size_t)(rb * 128 + row) * DM + lc * 8;
  }
  const u16* wsrc[3];
#pragma unroll
  for (int i = 0; i < 3; ++i) {
    int off = (wv * 3 + i) * 1024 + lane * 16;
    int row = off / 192, col = (off - row * 192) >> 1;
    int gc = oct0 * 8 + col;
    if (gc > FF - 8) gc = FF - 8;
    wsrc[i] = p.w1 + (size_t)row * FF + gc;
  }
  const unsigned lbase = __builtin_amdgcn_readfirstlane((unsigned)(size_t)(__attribute__((address_space(3))) char*)smem);
  auto issue = [&](int s) {
    unsigned xd = lbase + (s & (NSTG - 1)) * XSTG + __builtin_amdgcn_readfirstlane(wv) * 4096;
    unsigned wd = lbase + NSTG * XSTG + (s & (NSTG - 1)) * WSTG + __builtin_amdgcn_readfirstlane(wv) * 3072;
#pragma unroll
    for (int i = 0; i < 4; ++i) glds16(xsrc[i] + s * 64, xd + i * 1024);
#pragma unroll
    for (int i = 0; i < 3; ++i) glds16(wsrc[i] + (size_t)s * 64 * FF, wd + i * 1024);
  };
  issue(0); issue(1); issue(2);

  STAMP(0, 1);
  const int r = lane & 31, h = lane >> 5;
  f32x16 acc[3];
#pragma unroll
  for (int c = 0; c < 3; ++c)
#pragma unroll
    for (int i = 0; i < 16; ++i) acc[c][i] = 0.f;

  const int xrow = wv * 32 + r;
  const int xsw = (xrow >> 1) & 7;
  const int g16 = lane >> 4, q4 = (lane & 15) >> 2, pp = lane & 3;
  const int trrow = 8 * (g16 >> 1) + q4, trcol = 16 * (g16 & 1) + 4 * pp;

#pragma unroll
  for (int s = 0; s < 8; ++s) {
    if (s <= 4) asm volatile("s_waitcnt vmcnt(21)" ::: "memory");
    else if (s == 5) asm volatile("s_waitcnt vmcnt(14)" ::: "memory");
    else if (s == 6) asm volatile("s_waitcnt vmcnt(7)" ::: "memory");
    else asm volatile("s_waitcnt vmcnt(0)" ::: "memory");
    __builtin_amdgcn_s_barrier();
    if (s == 0) STAMP(0, 2);
    if (s + 3 < 8) issue(s + 3);
    const char* xb = xs + (s & (NSTG - 1)) * XSTG + xrow * 128;
    const char* wb = ws + (s & (NSTG - 1)) * WSTG;
#pragma unroll
    for (int ks = 0; ks < 4; ++ks) {
      bf16x8 xf = *(const bf16x8*)(xb + (((2 * ks + h) ^ xsw) << 4));
#pragma unroll
      for (int ct = 0; ct < 3; ++ct) {
        const char* a0 = wb + (16 * ks + trrow) * 192 + (32 * ct + trcol) * 2;
        s16x4 lo = __builtin_amdgcn_ds_read_tr16_b64_v4i16((__attribute__((address_space(3))) s16x4*)a0);
        s16x4 hi = __builtin_amdgcn_ds_read_tr16_b64_v4i16((__attribute__((address_space(3))) s16x4*)(a0 + 4 * 192));
        bf16x8 wf = __builtin_shufflevector(lo, hi, 0, 1, 2, 3, 4, 5, 6, 7);
        acc[ct] = __builtin_amdgcn_mfma_f32_32x32x16_bf16(wf, xf, acc[ct], 0, 0, 0);
      }
    }
  }

  STAMP(0, 3);
  const int R = rb * 4 + wv;
#pragma unroll
  for (int ct = 0; ct < 3; ++ct) {
#pragma unroll
    for (int gq = 0; gq < 4; ++gq) {
      int ol = 4 * ct + gq;
      if (ol < noct) {
        int o = oct0 + ol;
        u32x2 bb = b1r[ct][gq];
        float bv[4] = {bflo(bb[0]), bfhi(bb[0]), bflo(bb[1]), bfhi(bb[1])};
        float res[4];
#pragma unroll
        for (int i = 0; i < 4; ++i) {
          float hv = rbf(rbf(acc[ct][4 * gq + i]) + bv[i]);
          float e = __expf(fminf(hv, 30.f));
          float n = e * (e + 2.f);
          res[i] = hv * n * __builtin_amdgcn_rcpf(n + 2.f);
        }
        u32x2 o2 = {pk2(res[0], res[1]), pk2(res[2], res[3])};
        *(u32x2*)((char*)p.actF + (size_t)(R * NOCTP + o) * 512 + r * 16 + h * 8) = o2;
      }
    }
  }
  STAMP(0, 4);
}

#define CP_STRIDE 4160
#define CONV_U_OFF (2 * 8 * CP_STRIDE)
#define U_CH_STRIDE 7600
#define K2_LDS (CONV_U_OFF + 2 * U_CH_STRIDE)

__device__ __forceinline__ void conv_wg(const Params& p, int c, char* smem) {
  const int tid = threadIdx.x, lane = tid & 63, wv = tid >> 6;
  char* cps = smem;
  char* us = smem + CONV_U_OFF;
  STAMP(1, 0);

  u32x4 uv[2], flo[2], fhi[2];
#pragma unroll
  for (int i = 0; i < 2; ++i) {
    uv[i] = *(const u32x4*)(p.uT + (size_t)(2 * c + i) * SEQ + tid * 8);
    const u16* f = p.ssmk + (size_t)(2 * c + i) * SEQ;
    int i0 = 8 * tid - 8;
    u32x4 t = *(const u32x4*)(f + (i0 < 0 ? 0 : i0));
    flo[i] = i0 < 0 ? (u32x4){0u, 0u, 0u, 0u} : t;
    fhi[i] = *(const u32x4*)(f + 8 * tid);
  }
  for (int z = tid; z < 2 * 155; z += 256) {
    int ch = z / 155, zz = z - ch * 155;
    *(u32x4*)(us + ch * U_CH_STRIDE + zz * 16) = (u32x4){0u, 0u, 0u, 0u};
  }
  if (tid < 48) {
    int cpy = tid / 3, t = tid - cpy * 3;
    *(u32x4*)(cps + cpy * CP_STRIDE + t * 16) = (u32x4){0u, 0u, 0u, 0u};
  }
#pragma unroll
  for (int i = 0; i < 2; ++i)
    *(u32x4*)(us + i * U_CH_STRIDE + ((tid >> 2) + 31) * 80 + (tid & 3) * 16) = uv[i];
#pragma unroll
  for (int i = 0; i < 2; ++i) {
    unsigned wd[8] = {flo[i][0], flo[i][1], flo[i][2], flo[i][3], fhi[i][0], fhi[i][1], fhi[i][2], fhi[i][3]};
    unsigned rp[14];
#pragma unroll
    for (int j = -6; j <= 7; ++j) {
      if (j & 1) { unsigned x = wd[(j + 7) / 2]; rp[j + 6] = (x >> 16) | (x << 16); }
      else { rp[j + 6] = (wd[(j + 8) / 2] & 0xffffu) | (wd[(j + 8) / 2 - 1] & 0xffff0000u); }
    }
#pragma unroll
    for (int cc = 0; cc < 8; ++cc) {
      u32x4 o = {rp[cc + 6], rp[cc - 2 + 6], rp[cc - 4 + 6], rp[cc - 6 + 6]};
      *(u32x4*)(cps + (i * 8 + cc) * CP_STRIDE + (tid + 3) * 16) = o;
    }
  }
  __syncthreads();

  STAMP(1, 1);
  const int ch = wv >> 1, isB = wv & 1;
  const int r = lane & 31, h = lane >> 5;
  const char* ab = cps + (ch * 8 + (r & 7)) * CP_STRIDE + 16 * ((r >> 3) - h + 3);
  const char* ub = us + ch * U_CH_STRIDE + (r + 31) * 80 + 16 * h;

  f32x16 acc0, acc1;
#pragma unroll
  for (int i = 0; i < 16; ++i) { acc0[i] = 0.f; acc1[i] = 0.f; }

  auto run = [&](const char* a, const char* b, int n, f32x16& acc) {
    bf16x8 ca0 = *(const bf16x8*)(a - 32), cb0 = *(const bf16x8*)(b + 32);
    bf16x8 ca1 = *(const bf16x8*)(a), cb1 = *(const bf16x8*)(b);
#pragma unroll 4
    for (int qq = 0; qq < n; ++qq) {
      a += 64; b -= 80;
      bf16x8 na0 = *(const bf16x8*)(a - 32), nb0 = *(const bf16x8*)(b + 32);
      bf16x8 na1 = *(const bf16x8*)(a), nb1 = *(const bf16x8*)(b);
      SCHED_FENCE();
      acc = __builtin_amdgcn_mfma_f32_32x32x16_bf16(ca0, cb0, acc, 0, 0, 0);
      acc = __builtin_amdgcn_mfma_f32_32x32x16_bf16(ca1, cb1, acc, 0, 0, 0);
      SCHED_FENCE();
      ca0 = na0; cb0 = nb0; ca1 = na1; cb1 = nb1;
    }
  };
  if (isB) {
    run(ab, ub, 32, acc0);
    run(ab + 48 * 64, ub + 32 * 80 - 48 * 80, 16, acc1);
  } else {
    run(ab, ub + 32 * 80, 48, acc0);
  }
  __syncthreads();
  STAMP(1, 2);
  float* red = (float*)(smem + ch * 4096);
  if (isB) {
#pragma unroll
    for (int i = 0; i < 16; ++i) red[i * 64 + lane] = acc1[i];
  }
  __syncthreads();
  if (!isB) {
#pragma unroll
    for (int i = 0; i < 16; ++i) acc0[i] += red[i * 64 + lane];
  }
  u16* ost = (u16*)(smem + 8192) + ch * SEQ;
  {
    const int tau = isB ? 0 : 1;
#pragma unroll
    for (int gq = 0; gq < 4; ++gq) {
      u32x2 o = {pk2(acc0[4 * gq], acc0[4 * gq + 1]), pk2(acc0[4 * gq + 2], acc0[4 * gq + 3])};
      *(u32x2*)(ost + 1024 * tau + 32 * r + 8 * gq + 4 * h) = o;
    }
  }
  __syncthreads();
  {
    auto rsrc = __builtin_amdgcn_make_buffer_rsrc((void*)p.yT, 0, DM * SEQ * 2, 0x00020000);
#pragma unroll
    for (int i = 0; i < 2; ++i) {
      int x = tid + i * 256;
      int chh = x >> 8, xx = x & 255;
      u32x4 val = *(const u32x4*)((const u16*)(smem + 8192) + chh * SEQ + xx * 8);
      __builtin_amdgcn_raw_buffer_store_b128(val, rsrc, ((2 * c + chh) * SEQ + xx * 8) * 2, 0, 16);
    }
  }
  asm volatile("s_waitcnt vmcnt(0)" ::: "memory");
  __syncthreads();
  if (tid == 0) __hip_atomic_store(p.flags + c * 16, 1u, __ATOMIC_RELAXED, __HIP_MEMORY_SCOPE_AGENT);
  STAMP(1, 3);
}

#define RED_PITCH 68
__device__ __forceinline__ void gemm2_wg(const Params& p, int gi, char* smem) {
  const int tid = threadIdx.x, lane = tid & 63, wv = tid >> 6;
  const int grp = gi & 7, idx = gi >> 3;
  const int rb = 4 * grp + (idx >> 3);
  const int cb = idx & 7;

  STAMP(1, 0);
  const int erow = tid >> 2, ecol = (tid & 3) * 16;
  const int s = rb * 64 + erow, d0 = cb * 64 + ecol;
  u32x4 qv[2], kvv[2], dv[2];
#pragma unroll
  for (int i = 0; i < 2; ++i) {
    qv[i] = *(const u32x4*)(p.q + (size_t)s * DM + d0 + i * 8);
    kvv[i] = *(const u32x4*)(p.kvN + (size_t)s * DM + d0 + i * 8);
    dv[i] = *(const u32x4*)(p.ssmD + d0 + i * 8);
  }
  float gg = p.g[s];

  f32x16 acc[2][2];
#pragma unroll
  for (int a = 0; a < 2; ++a)
#pragma unroll
    for (int b = 0; b < 2; ++b)
#pragma unroll
      for (int i = 0; i < 16; ++i) acc[a][b][i] = 0.f;

  const char* ap = (const char*)p.actF + ((size_t)(2 * rb) * NOCTP + wv * 44) * 512 + lane * 16;
  const char* bp = (const char*)p.w2F + ((size_t)(2 * cb) * NOCTP + wv * 44) * 512 + lane * 16;
  constexpr int PF = 8;
  bf16x8 fa[PF][2], fb[PF][2];
#pragma unroll
  for (int i = 0; i < PF; ++i) {
    fa[i][0] = *(const bf16x8*)(ap + i * 1024);
    fa[i][1] = *(const bf16x8*)(ap + NOCTP * 512 + i * 1024);
    fb[i][0] = *(const bf16x8*)(bp + i * 1024);
    fb[i][1] = *(const bf16x8*)(bp + NOCTP * 512 + i * 1024);
  }
#pragma unroll
  for (int ks = 0; ks < 22; ++ks) {
    const int i = ks % PF;
    bf16x8 a0 = fa[i][0], a1 = fa[i][1], b0 = fb[i][0], b1 = fb[i][1];
    if (ks + PF < 22) {
      fa[i][0] = *(const bf16x8*)(ap + (ks + PF) * 1024);
      fa[i][1] = *(const bf16x8*)(ap + NOCTP * 512 + (ks + PF) * 1024);
      fb[i][0] = *(const bf16x8*)(bp + (ks + PF) * 1024);
      fb[i][1] = *(const bf16x8*)(bp + NOCTP * 512 + (ks + PF) * 1024);
    }
    SCHED_FENCE();
    acc[0][0] = __builtin_amdgcn_mfma_f32_32x32x16_bf16(a0, b0, acc[0][0], 0, 0, 0);
    acc[0][1] = __builtin_amdgcn_mfma_f32_32x32x16_bf16(a0, b1, acc[0][1], 0, 0, 0);
    acc[1][0] = __builtin_amdgcn_mfma_f32_32x32x16_bf16(a1, b0, acc[1][0], 0, 0, 0);
    acc[1][1] = __builtin_amdgcn_mfma_f32_32x32x16_bf16(a1, b1, acc[1][1], 0, 0, 0);
    SCHED_FENCE();
  }

  STAMP(1, 1);
  float* red = (float*)smem;
  const int r = lane & 31, h = lane >> 5;
#pragma unroll
  for (int a = 0; a < 2; ++a)
#pragma unroll
    for (int b = 0; b < 2; ++b)
#pragma unroll
      for (int i = 0; i < 16; ++i) {
        int m = a * 32 + (i & 3) + 8 * (i >> 2) + 4 * h;
        red[(wv * 64 + m) * RED_PITCH + b * 32 + r] = acc[a][b][i];
      }

  if (wv == 0) {
    const unsigned* fl = p.flags + (cb * 32 + (lane & 31)) * 16;
    for (int spin = 0; spin < (1 << 22); ++spin) {
      unsigned v = __hip_atomic_load(fl, __ATOMIC_RELAXED, __HIP_MEMORY_SCOPE_AGENT);
      if (__all(v != 0u)) break;
      __builtin_amdgcn_s_sleep(2);
    }
  }
  __syncthreads();
  __builtin_amdgcn_fence(__ATOMIC_ACQUIRE, "wavefront");
  STAMP(1, 2);

  u16* ys = (u16*)(smem + 4 * 64 * RED_PITCH * 4);
  {
    auto rsrc = __builtin_amdgcn_make_buffer_rsrc((void*)p.yT, 0, DM * SEQ * 2, 0x00020000);
    int dd = tid >> 2, qt = tid & 3;
    int goff = ((cb * 64 + dd) * SEQ + rb * 64 + qt * 16) * 2;
    u32x4 v0 = __builtin_amdgcn_raw_buffer_load_b128(rsrc, goff, 0, 16);
    u32x4 v1 = __builtin_amdgcn_raw_buffer_load_b128(rsrc, goff + 16, 0, 16);
    *(u32x4*)(ys + dd * 72 + qt * 16) = v0;
    *(u32x4*)(ys + dd * 72 + qt * 16 + 8) = v1;
  }
  __syncthreads();

  float bsum[16];
#pragma unroll
  for (int j = 0; j < 4; ++j) {
    f32x4 t = *(const f32x4*)(red + (0 * 64 + erow) * RED_PITCH + ecol + j * 4);
#pragma unroll
    for (int ww = 1; ww < 4; ++ww) t += *(const f32x4*)(red + (ww * 64 + erow) * RED_PITCH + ecol + j * 4);
#pragma unroll
    for (int i = 0; i < 4; ++i) bsum[j * 4 + i] = t[i];
  }
  const float omg = rbf(1.0f - gg);
  float res[16];
#pragma unroll
  for (int j = 0; j < 16; ++j) {
    float y = bflo((unsigned)ys[(ecol + j) * 72 + erow]);
    float kv = bfv(kvv[j >> 3], j & 7), dd = bfv(dv[j >> 3], j & 7), qq = bfv(qv[j >> 3], j & 7);
    float ysum = rbf(y + rbf(kv * dd));
    float a = rbf(qq * ysum);
    float b = rbf(bsum[j]);
    res[j] = rbf(gg * a) + rbf(omg * b);
  }
  u32x4 o0, o1;
#pragma unroll
  for (int j = 0; j < 4; ++j) { o0[j] = pk2(res[2 * j], res[2 * j + 1]); o1[j] = pk2(res[8 + 2 * j], res[8 + 2 * j + 1]); }
  *(u32x4*)(p.out + (size_t)s * DM + d0) = o0;
  *(u32x4*)(p.out + (size_t)s * DM + d0 + 8) = o1;
  STAMP(1, 3);
}

__global__ __launch_bounds__(256, 2) void k2_kernel(Params p) {
  __shared__ __attribute__((aligned(16))) char smem[K2_LDS];
  if (blockIdx.x < 256) conv_wg(p, blockIdx.x, smem);
  else gemm2_wg(p, blockIdx.x - 256, smem);
}

extern "C" void kernel_launch(void* const* d_in, const int* in_sizes, int n_in,
                              void* d_out, int out_size, void* d_ws, size_t ws_size,
                              hipStream_t stream) {
  Params p;
  p.q = (const u16*)d_in[0];
  p.k = (const u16*)d_in[1];
  p.v = (const u16*)d_in[2];
  p.shk = (const u16*)d_in[3];
  p.ssmk = (const u16*)d_in[4];
  p.ssmD = (const u16*)d_in[5];
  p.w1 = (const u16*)d_in[7];
  p.b1 = (const u16*)d_in[8];
  p.w2 = (const u16*)d_in[9];
  p.gw = (const u16*)d_in[10];
  p.out = (u16*)d_out;
  char* ws = (char*)d_ws;
  size_t off = 0;
  p.uT = (u16*)(ws + off);   off += (size_t)DM * SEQ * 2;
  p.kvN = (u16*)(ws + off);  off += (size_t)DM * SEQ * 2;
  p.yT = (u16*)(ws + off);   off += (size_t)DM * SEQ * 2;
  p.actF = (u16*)(ws + off); off += (size_t)64 * NOCTP * 512;
  p.w2F = (u16*)(ws + off);  off += (size_t)16 * NOCTP * 512;
  p.g = (float*)(ws + off);  off += SEQ * 4;
  p.flags = (unsigned*)(ws + off); off += 256 * 64;
  k1_kernel<<<256, 512, 0, stream>>>(p);
  k2_kernel<<<512, 256, 0, stream>>>(p);
}
```
